```python
import torch

def _softmax(x, axis=-1):
    return torch.softmax(x, dim=axis)
def _gelu(x):
    return torch.nn.functional.gelu(x, approximate="tanh")
def _silu(x):
    return torch.nn.functional.silu(x)
def _sigmoid(x):
    return torch.sigmoid(x)
def _layernorm(x, axis=-1, eps=1e-5):
    m = x.mean(dim=axis, keepdim=True)
    v = x.var(dim=axis, keepdim=True, unbiased=False)
    return (x - m) / torch.sqrt(v + eps)
def _scatter_add(tgt, idx, src, axis=0):
    return tgt.clone().index_add_(axis, idx, src)
def _segment_sum(src, seg, n):
    out = torch.zeros((n,) + src.shape[1:], dtype=src.dtype)
    return out.index_add_(0, seg, src)
def _pad(x, pw):
    flat = tuple(v for pair in reversed(pw) for v in pair)
    return torch.nn.functional.pad(x, flat)

kernel_name = 'standalone__geglu_ffn__prompt'

def setup_inputs():

    _w = (lambda a: (lambda t: t.to(torch.bfloat16) if t.is_floating_point() else t)(torch.as_tensor(a)))
    B, S, D, F = 1, 2048, 1024, 3584
    return {
        "x": _w((lambda *s: torch.randn(*s, dtype=torch.bfloat16) * 0.05)(B, S, D)),
        "w_gate": _w((lambda *s: torch.randn(*s, dtype=torch.bfloat16) * 0.05)(D, F)),
        "w_up": _w((lambda *s: torch.randn(*s, dtype=torch.bfloat16) * 0.05)(D, F)),
        "w_down": _w((lambda *s: torch.randn(*s, dtype=torch.bfloat16) * 0.05)(F, D)),
    }

def reference(x, w_gate, w_up, w_down):
    return (_gelu(x @ w_gate) * (x @ w_up)) @ w_down
```

```cpp
#include <hip/hip_runtime.h>
#include <hip/hip_bf16.h>
#include <stdint.h>

typedef short bf16x8 __attribute__((ext_vector_type(8)));
typedef short bf16x4 __attribute__((ext_vector_type(4)));
typedef float f32x4 __attribute__((ext_vector_type(4)));
typedef float f32x2 __attribute__((ext_vector_type(2)));
typedef unsigned u32x2 __attribute__((ext_vector_type(2)));
typedef unsigned u32x4 __attribute__((ext_vector_type(4)));
#define LAS __attribute__((address_space(3)))
typedef LAS unsigned char lds_u8;

constexpr int M_TOK = 2048, D_MODEL = 1024, F_FF = 3584;

#define WAIT_V(n) asm volatile("s_waitcnt vmcnt(%0)" ::"n"(n) : "memory")
#define WAIT_L0() asm volatile("s_waitcnt lgkmcnt(0)" ::: "memory")
#define BAR() __builtin_amdgcn_s_barrier()
#define SCHED() __builtin_amdgcn_sched_barrier(0)
#define FINL __device__ __forceinline__

template <int N> struct IC { static constexpr int value = N; };

FINL void glds16(const void* g, lds_u8* l) {
  __builtin_amdgcn_global_load_lds((const unsigned*)g, (LAS unsigned*)l, 16, 0, 0);
}
template <int OFF> FINL bf16x4 tr_read(unsigned addr) {
  bf16x4 r;
  asm volatile("ds_read_b64_tr_b16 %0, %1 offset:%2" : "=v"(r) : "v"(addr), "n"(OFF) : "memory");
  return r;
}
FINL bf16x8 cat8(bf16x4 a, bf16x4 b) { return __builtin_shufflevector(a, b, 0, 1, 2, 3, 4, 5, 6, 7); }
FINL unsigned cvt_pk_bf16(float lo, float hi) {
  unsigned r;
  asm("v_cvt_pk_bf16_f32 %0, %1, %2" : "=v"(r) : "v"(lo), "v"(hi));
  return r;
}
FINL f32x2 geglu2(f32x2 g, f32x2 u) {
  const float c0 = -2.0f * 0.7978845608028654f * 1.4426950408889634f;
  const float c1 = c0 * 0.044715f;
  f32x2 t = g * g;
  f32x2 p = t * c1 + c0;
  f32x2 a = g * p;
  f32x2 e;
  e.x = __builtin_amdgcn_exp2f(a.x);
  e.y = __builtin_amdgcn_exp2f(a.y);
  f32x2 d = e + 1.0f;
  f32x2 r;
  r.x = __builtin_amdgcn_rcpf(d.x);
  r.y = __builtin_amdgcn_rcpf(d.y);
  return (g * r) * u;
}
FINL u32x2 geglu4(f32x4 g, f32x4 u) {
  f32x2 a = geglu2((f32x2){g[0], g[1]}, (f32x2){u[0], u[1]});
  f32x2 b = geglu2((f32x2){g[2], g[3]}, (f32x2){u[2], u[3]});
  u32x2 o;
  o.x = cvt_pk_bf16(a.x, a.y);
  o.y = cvt_pk_bf16(b.x, b.y);
  return o;
}

constexpr int G1_WGRP = 2048;
constexpr int G1_WSLOT = 8 * G1_WGRP;
constexpr int G1_NW = 4;
constexpr int G1_XSLOT = 32768;
constexpr int G1_NX = 3;
constexpr int G1_XOFF = G1_NW * G1_WSLOT;
constexpr int G1_LDS = G1_XOFF + G1_NX * G1_XSLOT;

__global__ __launch_bounds__(512, 2) void k_gemm1(const unsigned char* __restrict__ x, const unsigned char* __restrict__ wg,
                                                  const unsigned char* __restrict__ wu, unsigned char* __restrict__ hbuf) {
  extern __shared__ __attribute__((aligned(16))) unsigned char smem_raw[];
  lds_u8* lds = (lds_u8*)smem_raw;
  const int tid = threadIdx.x;
  const int wid = __builtin_amdgcn_readfirstlane(tid >> 6);
  const int lane = tid & 63;
  const int half = wid >> 2, wm = wid & 3, wn = wid >> 2;
  const int bid = blockIdx.x;
  const int xcd = bid & 7, idx = bid >> 3;
  const int mt = (xcd >> 2) * 4 + (idx & 3);
  const int nt = (xcd & 3) * 8 + (idx >> 2);
  const int g = lane >> 4, q = (lane >> 2) & 3, p = lane & 3, tok = lane & 15;

  int li = lane - ((wid >> 1) & 1) * 8;
  li = (li < 0 || li >= 56) ? 0 : li;
  const unsigned voffW = (unsigned)((4 * wid + li / 14) * (F_FF * 2) + (li % 14) * 16);
  unsigned voffX[2];
#pragma unroll
  for (int i = 0; i < 2; ++i)
    voffX[i] = (unsigned)((8 * (2 * wid + i) + (lane >> 3)) * (D_MODEL * 2) + (((lane & 7) ^ ((i << 2) | (lane >> 4))) * 16));
  const unsigned char* wgb = wg + (size_t)nt * 224;
  const unsigned char* wub = wu + (size_t)nt * 224;
  const unsigned char* xb = x + (size_t)mt * 256 * (D_MODEL * 2);
  const int wdst = wid * G1_WGRP;
  const int xdst = G1_XOFF + wid * 2048;

  auto stageW = [&](int j, int slot) __attribute__((always_inline)) {
    const size_t go = (size_t)j * (32 * F_FF * 2);
    glds16(wgb + go + voffW, lds + slot * G1_WSLOT + wdst);
    glds16(wub + go + voffW, lds + slot * G1_WSLOT + wdst + 1024);
  };
  auto stageX = [&](int t, int hb, int slot) __attribute__((always_inline)) {
    const size_t go = (size_t)hb * (128 * D_MODEL * 2) + (size_t)t * 128;
    glds16(xb + go + voffX[0], lds + xdst + slot * G1_XSLOT + hb * 16384);
    glds16(xb + go + voffX[1], lds + xdst + slot * G1_XSLOT + hb * 16384 + 1024);
  };

  const unsigned lbase = (unsigned)(size_t)lds;
  const unsigned wrd = lbase + 2 * g * G1_WGRP + (g & 1) * 128 + q * 224 + p * 8 + wn * 128;
  const unsigned wrdm = lbase + 2 * g * G1_WGRP + (g & 1) * 128 + q * 224 + 96 + wn * 16 + (p & 1) * 8 + (p >> 1) * 1024;
  int xrd[2];
#pragma unroll
  for (int s = 0; s < 2; ++s) xrd[s] = G1_XOFF + (wm * 64 + tok) * 128 + ((((s << 2) | g) ^ ((tok >> 1) & 7)) * 16);

  f32x4 accg[4][3], accu[4][3], accm[4];
#pragma unroll
  for (int t = 0; t < 4; ++t) {
#pragma unroll
    for (int u = 0; u < 3; ++u) { accg[t][u] = (f32x4){0.f, 0.f, 0.f, 0.f}; accu[t][u] = (f32x4){0.f, 0.f, 0.f, 0.f}; }
    accm[t] = (f32x4){0.f, 0.f, 0.f, 0.f};
  }

  stageX(0, 0, 0); stageX(0, 1, 0); stageW(0, 0);
  stageW(1, 1); stageX(1, 0, 1);
  stageW(2, 2); stageX(1, 1, 1);
  WAIT_V(8);
  BAR();
  if (half) BAR();

  int xs_rd = 0, xs_wr = 2;
  auto step = [&](int j, auto S_, auto IW_, auto IX_, auto VM_) __attribute__((always_inline)) {
    constexpr int S = decltype(S_)::value;
    constexpr int IW = decltype(IW_)::value, IX = decltype(IX_)::value, VM = decltype(VM_)::value;
    bf16x8 xf[4], wgf[3], wuf[3], wmf;
    bf16x4 a0, a1, a2, a3, a4, a5, b0, b1, b2, b3, b4, b5, m0, m1;
    {
      const unsigned wb = (j & 3) * G1_WSLOT + wrd;
      const unsigned wbm = (j & 3) * G1_WSLOT + wrdm;
      lds_u8* xbp = lds + xs_rd * G1_XSLOT + xrd[S];
      a0 = tr_read<0>(wb); a1 = tr_read<G1_WGRP>(wb);
      b0 = tr_read<1024>(wb); b1 = tr_read<1024 + G1_WGRP>(wb);
#pragma unroll
      for (int t = 0; t < 4; ++t) xf[t] = *(LAS bf16x8*)(xbp + t * 2048);
      a2 = tr_read<32>(wb); a3 = tr_read<32 + G1_WGRP>(wb);
      b2 = tr_read<32 + 1024>(wb); b3 = tr_read<32 + 1024 + G1_WGRP>(wb);
      a4 = tr_read<64>(wb); a5 = tr_read<64 + G1_WGRP>(wb);
      b4 = tr_read<64 + 1024>(wb); b5 = tr_read<64 + 1024 + G1_WGRP>(wb);
      m0 = tr_read<0>(wbm); m1 = tr_read<G1_WGRP>(wbm);
    }
    if (IW) stageW(j + 3, (j + 3) & 3);
    if (IX) stageX((j >> 1) + 2, S, xs_wr);
    if (VM >= 0) WAIT_V(VM);
    asm volatile("s_waitcnt lgkmcnt(0)"
                 : "+v"(a0), "+v"(a1), "+v"(a2), "+v"(a3), "+v"(a4), "+v"(a5), "+v"(b0), "+v"(b1), "+v"(b2), "+v"(b3), "+v"(b4),
                   "+v"(b5), "+v"(m0), "+v"(m1)::"memory");
    wgf[0] = cat8(a0, a1); wgf[1] = cat8(a2, a3); wgf[2] = cat8(a4, a5);
    wuf[0] = cat8(b0, b1); wuf[1] = cat8(b2, b3); wuf[2] = cat8(b4, b5);
    wmf = cat8(m0, m1);
    BAR();
    SCHED();
    __builtin_amdgcn_s_setprio(1);
#pragma unroll
    for (int t = 0; t < 4; ++t) {
#pragma unroll
      for (int u = 0; u < 3; ++u) {
        accg[t][u] = __builtin_amdgcn_mfma_f32_16x16x32_bf16(wgf[u], xf[t], accg[t][u], 0, 0, 0);
        accu[t][u] = __builtin_amdgcn_mfma_f32_16x16x32_bf16(wuf[u], xf[t], accu[t][u], 0, 0, 0);
      }
      accm[t] = __builtin_amdgcn_mfma_f32_16x16x32_bf16(wmf, xf[t], accm[t], 0, 0, 0);
    }
    __builtin_amdgcn_s_setprio(0);
    SCHED();
    BAR();
    if (S) {
      xs_rd = (xs_rd == 2) ? 0 : xs_rd + 1;
      xs_wr = (xs_wr == 2) ? 0 : xs_wr + 1;
    }
  };
  for (int j = 0; j < 28; j += 2) {
    step(j, IC<0>{}, IC<1>{}, IC<1>{}, IC<8>{});
    step(j + 1, IC<1>{}, IC<1>{}, IC<1>{}, IC<8>{});
  }
  step(28, IC<0>{}, IC<1>{}, IC<0>{}, IC<6>{});
  step(29, IC<1>{}, IC<0>{}, IC<0>{}, IC<2>{});
  step(30, IC<0>{}, IC<0>{}, IC<0>{}, IC<0>{});
  step(31, IC<1>{}, IC<0>{}, IC<0>{}, IC<-1>{});
  if (!half) BAR();

  const int T0 = mt * 16 + wm * 4;
#pragma unroll
  for (int u = 0; u < 3; ++u) {
    const int ft = nt * 7 + wn * 4 + u;
    unsigned char* hp = hbuf + ((size_t)(ft >> 1) * 128 + T0) * 1024 + lane * 16 + (ft & 1) * 8;
#pragma unroll
    for (int t = 0; t < 4; ++t) *(u32x2*)(hp + t * 1024) = geglu4(accg[t][u], accu[t][u]);
  }
  {
    const int ft = nt * 7 + 3;
    unsigned char* hp = hbuf + ((size_t)(ft >> 1) * 128 + T0 + (lane >> 5)) * 1024 + ((2 * wn + (g & 1)) * 16 + tok) * 16 + (ft & 1) * 8;
#pragma unroll
    for (int t = 0; t < 4; t += 2) {
      f32x4 gg, uu;
#pragma unroll
      for (int r = 0; r < 4; ++r) {
        auto sw = __builtin_amdgcn_permlane32_swap(__float_as_uint(accm[t][r]), __float_as_uint(accm[t + 1][r]), false, false);
        gg[r] = __uint_as_float(sw[0]);
        uu[r] = __uint_as_float(sw[1]);
      }
      *(u32x2*)(hp + t * 1024) = geglu4(gg, uu);
    }
  }
}

constexpr int G2_KST = 8192 + 4096;
constexpr int G2_STAGE = 2 * G2_KST;
constexpr int G2_R = 6, G2_D = 5, G2_L = 6;
constexpr int G2_LDS = G2_R * G2_STAGE;

__global__ __launch_bounds__(512, 2) void k_gemm2(const unsigned char* __restrict__ hbuf, const unsigned char* __restrict__ wd,
                                                  unsigned char* __restrict__ out) {
  extern __shared__ __attribute__((aligned(16))) unsigned char smem_raw[];
  lds_u8* lds = (lds_u8*)smem_raw;
  const int tid = threadIdx.x;
  const int wid = __builtin_amdgcn_readfirstlane(tid >> 6);
  const int lane = tid & 63;
  const int H = wid >> 2, r = wid & 3, kgl = r >> 1, wm = r & 1;
  const int bid = blockIdx.x;
  const int xcd = bid & 7, idx = bid >> 3;
  const int mt = (xcd >> 1) * 4 + (idx & 3);
  const int nt = (xcd & 1) * 8 + (idx >> 2);
  const int g = lane >> 4, q = (lane >> 2) & 3, p = lane & 3, tok = lane & 15;

  const unsigned voffH = (unsigned)(lane * 16);
  const int rl = lane >> 3;
  const unsigned voffD = (unsigned)((16 * (rl >> 2) + 4 * r + (rl & 3)) * (D_MODEL * 2) +
                                    (((((lane & 7) >> 1) ^ (((rl >> 1) & 1) | ((r & 1) << 1))) << 1) | (lane & 1)) * 16);
  const unsigned char* hb = hbuf + ((size_t)mt * 8 + 2 * r) * 1024;
  const unsigned char* db = wd + (size_t)nt * 128;
  auto stage = [&](int sg, int slot) __attribute__((always_inline)) {
#pragma unroll
    for (int i = 0; i < 2; ++i) {
      const int S = 2 * sg + i;
      lds_u8* l = lds + slot * G2_STAGE + i * G2_KST;
      glds16(hb + (size_t)S * (128 * 1024) + voffH, l + r * 2048);
      glds16(hb + (size_t)S * (128 * 1024) + 1024 + voffH, l + r * 2048 + 1024);
      glds16(db + (size_t)S * (32 * D_MODEL * 2) + voffD, l + 8192 + r * 1024);
    }
  };
  const int hrd = kgl * G2_KST + wm * 4096 + lane * 16;
  unsigned drd[4];
  {
    const unsigned lbase = (unsigned)(size_t)lds;
    const int f = ((q >> 1) & 1) | ((g & 1) << 1);
#pragma unroll
    for (int u = 0; u < 4; ++u) drd[u] = lbase + kgl * G2_KST + 8192 + (8 * g + q) * 128 + ((u ^ f) * 32) + 8 * p;
  }
  f32x4 acc[4][4];
#pragma unroll
  for (int t = 0; t < 4; ++t)
#pragma unroll
    for (int u = 0; u < 4; ++u) acc[t][u] = (f32x4){0.f, 0.f, 0.f, 0.f};

  if (H) {
#pragma unroll
    for (int s = 0; s < G2_D; s += 2) stage(s, s);
    WAIT_V(G2_L * (G2_D - 1) / 2);
  } else {
#pragma unroll
    for (int s = 1; s < G2_D; s += 2) stage(s, s);
  }
  BAR();
  if (H) BAR();

  int rs = H;
  int ws = (H + G2_D) % G2_R;
  auto step = [&](int it, auto VM_) __attribute__((always_inline)) {
    constexpr int VM = decltype(VM_)::value;
    const int sg = 2 * it + H;
    bf16x8 hf[4], df[4];
    bf16x4 d0, d1, d2, d3, d4, d5, d6, d7;
    {
      lds_u8* b = lds + rs * G2_STAGE;
      const unsigned so = rs * G2_STAGE;
      d0 = tr_read<0>(drd[0] + so); d1 = tr_read<512>(drd[0] + so);
#pragma unroll
      for (int t = 0; t < 4; ++t) hf[t] = *(LAS bf16x8*)(b + hrd + t * 1024);
      d2 = tr_read<0>(drd[1] + so); d3 = tr_read<512>(drd[1] + so);
      d4 = tr_read<0>(drd[2] + so); d5 = tr_read<512>(drd[2] + so);
      d6 = tr_read<0>(drd[3] + so); d7 = tr_read<512>(drd[3] + so);
    }
    if (sg + G2_D < 56) stage(sg + G2_D, ws);
    WAIT_V(VM);
    asm volatile("s_waitcnt lgkmcnt(0)" : "+v"(d0), "+v"(d1), "+v"(d2), "+v"(d3), "+v"(d4), "+v"(d5), "+v"(d6), "+v"(d7)::"memory");
    df[0] = cat8(d0, d1); df[1] = cat8(d2, d3); df[2] = cat8(d4, d5); df[3] = cat8(d6, d7);
    BAR();
    SCHED();
    __builtin_amdgcn_s_setprio(1);
#pragma unroll
    for (int t = 0; t < 4; ++t)
#pragma unroll
      for (int u = 0; u < 4; ++u) acc[t][u] = __builtin_amdgcn_mfma_f32_16x16x32_bf16(df[u], hf[t], acc[t][u], 0, 0, 0);
    __builtin_amdgcn_s_setprio(0);
    SCHED();
    BAR();
    rs += 2; if (rs >= G2_R) rs -= G2_R;
    ws += 2; if (ws >= G2_R) ws -= G2_R;
  };
  for (int it = 0; it <= 26 - (G2_D - 1) / 2; ++it) step(it, IC<G2_L * (G2_D - 1) / 2>{});
  step(25, IC<G2_L * 1>{});
  step(26, IC<0>{});
  step(27, IC<0>{});
  if (!H) BAR();

  const int c = H * 2 + kgl;
  {
#pragma unroll
    for (int t = 0; t < 4; ++t) {
      if (t != c) {
#pragma unroll
        for (int u = 0; u < 4; ++u) *(LAS f32x4*)(lds + ((((wm * 4 + t) * 4 + c) * 4 + u) * 1024) + lane * 16) = acc[t][u];
      }
    }
  }
  __syncthreads();
  f32x4 o[4];
#pragma unroll
  for (int u = 0; u < 4; ++u) {
    o[u] = (c == 0) ? acc[0][u] : (c == 1) ? acc[1][u] : (c == 2) ? acc[2][u] : acc[3][u];
  }
#pragma unroll
  for (int s = 1; s < 4; ++s) {
    const int sc = (c + s) & 3;
#pragma unroll
    for (int u = 0; u < 4; ++u) o[u] += *(LAS f32x4*)(lds + ((((wm * 4 + c) * 4 + sc) * 4 + u) * 1024) + lane * 16);
  }
  unsigned char* op = out + ((size_t)(mt * 128 + wm * 64 + c * 16 + tok) * D_MODEL + nt * 64) * 2;
#pragma unroll
  for (int u = 0; u < 4; u += 2) {
    unsigned a0 = cvt_pk_bf16(o[u][0], o[u][1]), a1 = cvt_pk_bf16(o[u][2], o[u][3]);
    unsigned b0 = cvt_pk_bf16(o[u + 1][0], o[u + 1][1]), b1 = cvt_pk_bf16(o[u + 1][2], o[u + 1][3]);
    auto s0 = __builtin_amdgcn_permlane16_swap(a0, b0, false, false);
    auto s1 = __builtin_amdgcn_permlane16_swap(a1, b1, false, false);
    u32x4 v = {s0[0], s1[0], s0[1], s1[1]};
    const int col = (g & 1) ? (16 * (u + 1) + 4 * (g - 1)) : (16 * u + 4 * g);
    *(u32x4*)(op + col * 2) = v;
  }
}

extern "C" void kernel_launch(void* const* d_in, const int* in_sizes, int n_in, void* d_out, int out_size, void* d_ws,
                              size_t ws_size, hipStream_t stream) {
  const unsigned char* x = (const unsigned char*)d_in[0];
  const unsigned char* wg = (const unsigned char*)d_in[1];
  const unsigned char* wu = (const unsigned char*)d_in[2];
  const unsigned char* wd = (const unsigned char*)d_in[3];
  unsigned char* hbuf = (unsigned char*)d_ws;
  (void)hipFuncSetAttribute((const void*)k_gemm1, hipFuncAttributeMaxDynamicSharedMemorySize, G1_LDS);
  (void)hipFuncSetAttribute((const void*)k_gemm2, hipFuncAttributeMaxDynamicSharedMemorySize, G2_LDS);
  k_gemm1<<<256, 512, G1_LDS, stream>>>(x, wg, wu, hbuf);
  k_gemm2<<<256, 512, G2_LDS, stream>>>(hbuf, wd, (unsigned char*)d_out);
}
```

```cpp
#include <hip/hip_runtime.h>
#include <hip/hip_bf16.h>
#include <stdint.h>
#define PF3 1

typedef short bf16x8 __attribute__((ext_vector_type(8)));
typedef short bf16x4 __attribute__((ext_vector_type(4)));
typedef float f32x4 __attribute__((ext_vector_type(4)));
typedef float f32x2 __attribute__((ext_vector_type(2)));
typedef unsigned u32x2 __attribute__((ext_vector_type(2)));
typedef unsigned u32x4 __attribute__((ext_vector_type(4)));
#define LAS __attribute__((address_space(3)))
typedef LAS unsigned char lds_u8;

constexpr int M_TOK = 2048, D_MODEL = 1024, F_FF = 3584;
constexpr size_t WS_XCH = 16u << 20;
constexpr size_t WS_FLAG = 32u << 20;

#define WAIT_V(n) asm volatile("s_waitcnt vmcnt(%0)" ::"n"(n) : "memory")
#define WAIT_L0() asm volatile("s_waitcnt lgkmcnt(0)" ::: "memory")
#define BAR() __builtin_amdgcn_s_barrier()
#define SCHED() __builtin_amdgcn_sched_barrier(0)
#define FINL __device__ __forceinline__

#define STAMP_DECL
#define STAMP_NOW()
#define STAMP_OUT(base)
template <int N> struct IC { static constexpr int value = N; };

FINL void glds16(const void* g, lds_u8* l) {
  __builtin_amdgcn_global_load_lds((const unsigned*)g, (LAS unsigned*)l, 16, 0, 0);
}
template <int OFF> FINL bf16x4 tr_read(unsigned addr) {
  bf16x4 r;
  asm volatile("ds_read_b64_tr_b16 %0, %1 offset:%2" : "=v"(r) : "v"(addr), "n"(OFF) : "memory");
  return r;
}
template <int OFF> FINL bf16x8 ld128(unsigned addr) {
  bf16x8 r;
  asm volatile("ds_read_b128 %0, %1 offset:%2" : "=v"(r) : "v"(addr), "n"(OFF) : "memory");
  return r;
}
#define WAIT_L(n) asm volatile("s_waitcnt lgkmcnt(%0)" ::"n"(n) : "memory")
FINL bf16x8 cat8(bf16x4 a, bf16x4 b) { return __builtin_shufflevector(a, b, 0, 1, 2, 3, 4, 5, 6, 7); }
FINL unsigned cvt_pk_bf16(float lo, float hi) {
  unsigned r;
  asm("v_cvt_pk_bf16_f32 %0, %1, %2" : "=v"(r) : "v"(lo), "v"(hi));
  return r;
}
FINL f32x2 geglu2(f32x2 g, f32x2 u) {
  const float c0 = -2.0f * 0.7978845608028654f * 1.4426950408889634f;
  const float c1 = c0 * 0.044715f;
  f32x2 t = g * g;
  f32x2 p = t * c1 + c0;
  f32x2 a = g * p;
  f32x2 e;
  e.x = __builtin_amdgcn_exp2f(a.x);
  e.y = __builtin_amdgcn_exp2f(a.y);
  f32x2 d = e + 1.0f;
  f32x2 r;
  r.x = __builtin_amdgcn_rcpf(d.x);
  r.y = __builtin_amdgcn_rcpf(d.y);
  return (g * r) * u;
}
FINL u32x2 geglu4(f32x4 g, f32x4 u) {
  f32x2 a = geglu2((f32x2){g[0], g[1]}, (f32x2){u[0], u[1]});
  f32x2 b = geglu2((f32x2){g[2], g[3]}, (f32x2){u[2], u[3]});
  u32x2 o;
  o.x = cvt_pk_bf16(a.x, a.y);
  o.y = cvt_pk_bf16(b.x, b.y);
  return o;
}

constexpr int G1_WGRP = 2048;
constexpr int G1_WSLOT = 8 * G1_WGRP;
constexpr int G1_NW = 4;
constexpr int G1_XSLOT = 32768;
constexpr int G1_NX = 3;
constexpr int G1_XOFF = G1_NW * G1_WSLOT;
constexpr int G1_LDS = G1_XOFF + G1_NX * G1_XSLOT;

__global__ __launch_bounds__(512, 2) void k_gemm1(const unsigned char* __restrict__ x, const unsigned char* __restrict__ wg,
                                                  const unsigned char* __restrict__ wu, unsigned char* __restrict__ hbuf,
                                                  const unsigned char* __restrict__ wd) {
  extern __shared__ __attribute__((aligned(16))) unsigned char smem_raw[];
  lds_u8* lds = (lds_u8*)smem_raw;
  const int tid = threadIdx.x;
  const int wid = __builtin_amdgcn_readfirstlane(tid >> 6);
  const int lane = tid & 63;
  const int half = wid >> 2, wm = wid & 3, wn = wid >> 2;
  STAMP_DECL
  STAMP_NOW();
  const int bid = blockIdx.x;
  const int xcd = bid & 7, idx = bid >> 3;
  const int mt = (xcd >> 2) * 4 + (idx & 3);
  const int nt = (xcd & 3) * 8 + (idx >> 2);
  const int g = lane >> 4, q = (lane >> 2) & 3, p = lane & 3, tok = lane & 15;

  int li = lane - ((wid >> 1) & 1) * 8;
  li = (li < 0 || li >= 56) ? 0 : li;
  const unsigned voffW = (unsigned)((4 * wid + li / 14) * (F_FF * 2) + (li % 14) * 16);
  unsigned voffX[2];
#pragma unroll
  for (int i = 0; i < 2; ++i)
    voffX[i] = (unsigned)((8 * (2 * wid + i) + (lane >> 3)) * (D_MODEL * 2) + (((lane & 7) ^ ((i << 2) | (lane >> 4))) * 16));
  const unsigned char* wgb = wg + (size_t)nt * 224;
  const unsigned char* wub = wu + (size_t)nt * 224;
  const unsigned char* xb = x + (size_t)mt * 256 * (D_MODEL * 2);
  const int wdst = wid * G1_WGRP;
  const int xdst = G1_XOFF + wid * 2048;

  const int rot2 = 0;
  auto stageW = [&](int j, int slot) __attribute__((always_inline)) {
    const size_t go = (size_t)((j + 2 * rot2) & 31) * (32 * F_FF * 2);
    glds16(wgb + go + voffW, lds + slot * G1_WSLOT + wdst);
    glds16(wub + go + voffW, lds + slot * G1_WSLOT + wdst + 1024);
  };
  auto stageX = [&](int t, int hb, int slot) __attribute__((always_inline)) {
    const size_t go = (size_t)hb * (128 * D_MODEL * 2) + (size_t)((t + rot2) & 15) * 128;
    glds16(xb + go + voffX[0], lds + xdst + slot * G1_XSLOT + hb * 16384);
    glds16(xb + go + voffX[1], lds + xdst + slot * G1_XSLOT + hb * 16384 + 1024);
  };

  const unsigned lbase = (unsigned)(size_t)lds;
  const unsigned wrd = lbase + 2 * g * G1_WGRP + (g & 1) * 128 + q * 224 + p * 8 + wn * 128;
  const unsigned wrdm = lbase + 2 * g * G1_WGRP + (g & 1) * 128 + q * 224 + 96 + wn * 16 + (p & 1) * 8 + (p >> 1) * 1024;
  int xrd[2];
#pragma unroll
  for (int s = 0; s < 2; ++s) xrd[s] = G1_XOFF + (wm * 64 + tok) * 128 + ((((s << 2) | g) ^ ((tok >> 1) & 7)) * 16);

  f32x4 accg[4][3], accu[4][3], accm[4];
#pragma unroll
  for (int t = 0; t < 4; ++t) {
#pragma unroll
    for (int u = 0; u < 3; ++u) { accg[t][u] = (f32x4){0.f, 0.f, 0.f, 0.f}; accu[t][u] = (f32x4){0.f, 0.f, 0.f, 0.f}; }
    accm[t] = (f32x4){0.f, 0.f, 0.f, 0.f};
  }

  stageX(0, 0, 0); stageX(0, 1, 0); stageW(0, 0);
  stageW(1, 1); stageX(1, 0, 1);
  stageW(2, 2); stageX(1, 1, 1);
  stageW(3, 3); stageX(2, 0, 2);
  WAIT_V(12);
  BAR();
  STAMP_NOW();

  const unsigned xra[2] = {lbase + (unsigned)xrd[0], lbase + (unsigned)xrd[1]};
  bf16x4 wl[7], wh[7];
  bf16x8 xa[4], xb2[4];
  {
    wl[0] = tr_read<0>(wrd); wh[0] = tr_read<G1_WGRP>(wrd);
    wl[1] = tr_read<32>(wrd); wh[1] = tr_read<32 + G1_WGRP>(wrd);
    wl[2] = tr_read<64>(wrd); wh[2] = tr_read<64 + G1_WGRP>(wrd);
    wl[3] = tr_read<1024>(wrd); wh[3] = tr_read<1024 + G1_WGRP>(wrd);
    wl[4] = tr_read<1024 + 32>(wrd); wh[4] = tr_read<1024 + 32 + G1_WGRP>(wrd);
    wl[5] = tr_read<1024 + 64>(wrd); wh[5] = tr_read<1024 + 64 + G1_WGRP>(wrd);
    wl[6] = tr_read<0>(wrdm); wh[6] = tr_read<G1_WGRP>(wrdm);
    xa[0] = ld128<0>(xra[0]); xa[1] = ld128<2048>(xra[0]); xa[2] = ld128<4096>(xra[0]); xa[3] = ld128<6144>(xra[0]);
    WAIT_L(0);
  }
  int xs_rd = 0, xs_wr = 2;
  auto step = [&](int k, auto S_, auto IW_, auto IX_, auto VM_, auto RD_, bf16x8(&xc)[4], bf16x8(&xn)[4], auto IP_) __attribute__((always_inline)) {
    constexpr int S = decltype(S_)::value;
    constexpr int IP = decltype(IP_)::value;
    constexpr int IW = decltype(IW_)::value, IX = decltype(IX_)::value, VM = decltype(VM_)::value, RD = decltype(RD_)::value;
    if (S) xs_rd = (xs_rd == 2) ? 0 : xs_rd + 1;
    const unsigned wb = ((k + 1) & 3) * G1_WSLOT + wrd;
    const unsigned wbm = ((k + 1) & 3) * G1_WSLOT + wrdm;
    const unsigned xbp = xs_rd * G1_XSLOT + xra[S ^ 1];
#define MF(ACC, WF, XF) ACC = __builtin_amdgcn_mfma_f32_16x16x32_bf16(WF, XF, ACC, 0, 0, 0); SCHED();
    constexpr int LW = IW, LX = IX;
    const size_t goW = (size_t)((k + 4 + 2 * rot2) & 31) * (32 * F_FF * 2);
    const size_t goX = (size_t)(S ^ 1) * (128 * D_MODEL * 2) + (size_t)((((k + 1) >> 1) + 2 + rot2) & 15) * 128;
    lds_u8* ldW = lds + (k & 3) * G1_WSLOT + wdst;
    lds_u8* ldX = lds + xdst + xs_wr * G1_XSLOT + (S ^ 1) * 16384;
    bf16x8 wf;
    WAIT_L(10);
    SCHED();
    wf = cat8(wl[0], wh[0]);
    MF(accg[0][0], wf, xc[0]);
    MF(accg[1][0], wf, xc[1]);
    MF(accg[2][0], wf, xc[2]);
    MF(accg[3][0], wf, xc[3]);
    WAIT_L(0);
    if (RD) {
      if (VM >= 0) WAIT_V(VM);
      BAR();
    }
    SCHED();
    wf = cat8(wl[3], wh[3]);
    MF(accu[0][0], wf, xc[0]);
    if (RD) wl[0] = tr_read<0>(wb);
    SCHED();
    MF(accu[1][0], wf, xc[1]);
    if (RD) wh[0] = tr_read<0 + G1_WGRP>(wb);
    SCHED();
    MF(accu[2][0], wf, xc[2]);
    if (RD) xn[0] = ld128<0>(xbp);
    SCHED();
    MF(accu[3][0], wf, xc[3]);
    if (RD) xn[1] = ld128<2048>(xbp);
    SCHED();
    wf = cat8(wl[1], wh[1]);
    MF(accg[0][1], wf, xc[0]);
    if (RD) wl[3] = tr_read<1024>(wb);
    SCHED();
    MF(accg[1][1], wf, xc[1]);
    if (RD) wh[3] = tr_read<1024 + G1_WGRP>(wb);
    SCHED();
    MF(accg[2][1], wf, xc[2]);
    if (RD) xn[2] = ld128<4096>(xbp);
    SCHED();
    MF(accg[3][1], wf, xc[3]);
    if (RD) xn[3] = ld128<6144>(xbp);
    SCHED();
    wf = cat8(wl[4], wh[4]);
    MF(accu[0][1], wf, xc[0]);
    if (RD) wl[1] = tr_read<32>(wb);
    SCHED();
    MF(accu[1][1], wf, xc[1]);
    if (RD) wh[1] = tr_read<32 + G1_WGRP>(wb);
    SCHED();
    MF(accu[2][1], wf, xc[2]);
    if (LW) glds16(wgb + goW + voffW, ldW);
    SCHED();
    MF(accu[3][1], wf, xc[3]);
    wf = cat8(wl[2], wh[2]);
    MF(accg[0][2], wf, xc[0]);
    if (RD) wl[4] = tr_read<1024 + 32>(wb);
    SCHED();
    MF(accg[1][2], wf, xc[1]);
    if (RD) wh[4] = tr_read<1024 + 32 + G1_WGRP>(wb);
    SCHED();
    MF(accg[2][2], wf, xc[2]);
    if (LW) glds16(wub + goW + voffW, ldW + 1024);
    SCHED();
    MF(accg[3][2], wf, xc[3]);
    wf = cat8(wl[5], wh[5]);
    MF(accu[0][2], wf, xc[0]);
    if (RD) wl[2] = tr_read<64>(wb);
    SCHED();
    MF(accu[1][2], wf, xc[1]);
    if (RD) wh[2] = tr_read<64 + G1_WGRP>(wb);
    SCHED();
    MF(accu[2][2], wf, xc[2]);
    if (LX) glds16(xb + goX + voffX[0], ldX);
    SCHED();
    MF(accu[3][2], wf, xc[3]);
    wf = cat8(wl[6], wh[6]);
    MF(accm[0], wf, xc[0]);
    if (RD) wl[5] = tr_read<1024 + 64>(wb);
    SCHED();
    MF(accm[1], wf, xc[1]);
    if (RD) wh[5] = tr_read<1024 + 64 + G1_WGRP>(wb);
    SCHED();
    MF(accm[2], wf, xc[2]);
    if (LX) glds16(xb + goX + voffX[1], ldX + 1024);
    SCHED();
    MF(accm[3], wf, xc[3]);
    if (RD) wl[6] = tr_read<0>(wbm);
    if (RD) wh[6] = tr_read<0 + G1_WGRP>(wbm);
    SCHED();
    if (!S) xs_wr = (xs_wr == 2) ? 0 : xs_wr + 1;
  };
  for (int k = 0; k < 26; k += 2) {
    step(k, IC<0>{}, IC<1>{}, IC<1>{}, IC<8>{}, IC<1>{}, xa, xb2, IC<0>{});
    step(k + 1, IC<1>{}, IC<1>{}, IC<1>{}, IC<8>{}, IC<1>{}, xb2, xa, IC<0>{});
  }
  step(26, IC<0>{}, IC<1>{}, IC<1>{}, IC<8>{}, IC<1>{}, xa, xb2, IC<0>{});
  step(27, IC<1>{}, IC<1>{}, IC<0>{}, IC<8>{}, IC<1>{}, xb2, xa, IC<0>{});
  step(28, IC<0>{}, IC<0>{}, IC<0>{}, IC<6>{}, IC<1>{}, xa, xb2, IC<0>{});
  step(29, IC<1>{}, IC<0>{}, IC<0>{}, IC<2>{}, IC<1>{}, xb2, xa, IC<0>{});
  step(30, IC<0>{}, IC<0>{}, IC<0>{}, IC<0>{}, IC<1>{}, xa, xb2, IC<0>{});
  unsigned pf3;
  {
    const int e2 = (wid < 7 ? wid : 6) * 64 + lane;
    const unsigned char* pw = wd + (size_t)(((xcd >> 1) & 1) * 1792 + idx * 56 + (e2 >> 3)) * 2048 + (xcd & 1) * 1024 + (e2 & 7) * 128;
    asm volatile("global_load_dword %0, %1, off" : "=v"(pf3) : "v"(pw) : "memory");
  }
  step(31, IC<1>{}, IC<0>{}, IC<0>{}, IC<-1>{}, IC<0>{}, xb2, xa, IC<0>{});
  STAMP_NOW();

  if (lane == 0) *(unsigned*)(hbuf + WS_FLAG + (size_t)(blockIdx.x * 8 + wid) * 128) = 0u;
  const int T0 = mt * 16 + wm * 4;
#pragma unroll
  for (int u = 0; u < 3; ++u) {
    const int ft = nt * 7 + wn * 4 + u;
    unsigned char* hp = hbuf + ((size_t)(ft >> 1) * 128 + T0) * 1024 + lane * 16 + (ft & 1) * 8;
#pragma unroll
    for (int t = 0; t < 4; ++t) *(u32x2*)(hp + t * 1024) = geglu4(accg[t][u], accu[t][u]);
  }
  {
    const int ft = nt * 7 + 3;
    unsigned char* hp = hbuf + ((size_t)(ft >> 1) * 128 + T0 + (lane >> 5)) * 1024 + ((2 * wn + (g & 1)) * 16 + tok) * 16 + (ft & 1) * 8;
#pragma unroll
    for (int t = 0; t < 4; t += 2) {
      f32x4 gg, uu;
#pragma unroll
      for (int r = 0; r < 4; ++r) {
        auto sw = __builtin_amdgcn_permlane32_swap(__float_as_uint(accm[t][r]), __float_as_uint(accm[t + 1][r]), false, false);
        gg[r] = __uint_as_float(sw[0]);
        uu[r] = __uint_as_float(sw[1]);
      }
      *(u32x2*)(hp + t * 1024) = geglu4(gg, uu);
    }
  }
  STAMP_NOW();
  WAIT_V(0);
  asm volatile("" ::"v"(pf3));
  STAMP_NOW();
  STAMP_OUT(hbuf + (64u << 20));
}

constexpr int G2_STAGE = 16384;
#define G2_R 10
constexpr int G2_D = G2_R - 1, G2_L = 4;
constexpr int G2_LDS = G2_R * G2_STAGE;

__global__ __launch_bounds__(512, 2) void k_gemm2(unsigned char* __restrict__ hbuf, const unsigned char* __restrict__ wd,
                                                  unsigned char* __restrict__ out) {
  extern __shared__ __attribute__((aligned(16))) unsigned char smem_raw[];
  lds_u8* lds = (lds_u8*)smem_raw;
  const int tid = threadIdx.x;
  const int wid = __builtin_amdgcn_readfirstlane(tid >> 6);
  const int lane = tid & 63;
  const int H = wid >> 2, r = wid & 3, wm = r & 1, wn = r >> 1;
  STAMP_DECL
  STAMP_NOW();
  const int bid = blockIdx.x;
  const int xcd = bid & 7, idx = bid >> 3;
  const int kh = (xcd >> 1) & 1;
  const int mt = (xcd >> 2) * 8 + (idx & 7);
  const int nt = (xcd & 1) * 4 + (idx >> 3);
  const int g = lane >> 4, q = (lane >> 2) & 3, p = lane & 3, tok = lane & 15;

  const unsigned voffH = (unsigned)(lane * 16);
  const unsigned voffD = (unsigned)((4 * r + g) * (D_MODEL * 2) + ((((tok >> 1) ^ (g | ((r & 1) << 2))) << 1) | (lane & 1)) * 16);
  const unsigned char* hb = hbuf + ((size_t)kh * 56 * 128 + mt * 8 + 2 * r) * 1024;
  const unsigned char* db = wd + (size_t)kh * 56 * 32 * (D_MODEL * 2) + (size_t)nt * 256;
  auto stage = [&](int S, int slot) __attribute__((always_inline)) {
    lds_u8* l = lds + slot * G2_STAGE;
    glds16(hb + (size_t)S * (128 * 1024) + voffH, l + r * 2048);
    glds16(hb + (size_t)S * (128 * 1024) + 1024 + voffH, l + r * 2048 + 1024);
    glds16(db + (size_t)S * (32 * D_MODEL * 2) + voffD, l + 8192 + r * 2048);
    glds16(db + (size_t)S * (32 * D_MODEL * 2) + 16 * D_MODEL * 2 + voffD, l + 8192 + r * 2048 + 1024);
  };
  const int hrd = wm * 4096 + lane * 16;
  unsigned drd[4];
  {
    const unsigned lbase = (unsigned)(size_t)lds;
    const int f = q | ((g & 1) << 2);
#pragma unroll
    for (int u = 0; u < 4; ++u) drd[u] = lbase + 8192 + (8 * g + q) * 256 + (((4 * wn + u) ^ f) * 32) + 8 * p;
  }
  f32x4 acc[4][4];
#pragma unroll
  for (int t = 0; t < 4; ++t)
#pragma unroll
    for (int u = 0; u < 4; ++u) acc[t][u] = (f32x4){0.f, 0.f, 0.f, 0.f};

  const unsigned lbase2 = (unsigned)(size_t)lds;
  const unsigned hra = lbase2 + H * G2_STAGE + hrd;
#pragma unroll
  for (int u = 0; u < 4; ++u) drd[u] += H * G2_STAGE;
#pragma unroll
  for (int i = 0; i < 5; ++i) stage(2 * i + H, 2 * i + H);
  WAIT_V(16);
  BAR();
  STAMP_NOW();
  bf16x4 dla[4], dha[4], dlb[4], dhb[4];
  bf16x8 ha[4], hb2[4];
  {
#pragma unroll
    for (int u = 0; u < 4; ++u) { dla[u] = tr_read<0>(drd[u]); dha[u] = tr_read<1024>(drd[u]); }
    ha[0] = ld128<0>(hra); ha[1] = ld128<1024>(hra); ha[2] = ld128<2048>(hra); ha[3] = ld128<3072>(hra);
  }
  int rs = 1, ws = 0;
  auto step = [&](int i, auto IL_, auto VM_, auto RD_, bf16x8(&hc)[4], bf16x8(&hn)[4], bf16x4(&dl)[4], bf16x4(&dh)[4], bf16x4(&dln)[4],
                  bf16x4(&dhn)[4]) __attribute__((always_inline)) {
    constexpr int IL = decltype(IL_)::value, VM = decltype(VM_)::value, RD = decltype(RD_)::value;
    const unsigned so = rs * (2 * G2_STAGE);
    const int S = 2 * (i + 5) + H;
    lds_u8* l = lds + ws * (2 * G2_STAGE) + H * G2_STAGE;
    constexpr int LL = IL;
    bf16x8 df;
    WAIT_L(0);
    if (RD) {
      WAIT_V(VM);
      BAR();
    }
    SCHED();
    df = cat8(dl[0], dh[0]);
    MF(acc[0][0], df, hc[0]);
    if (RD) dln[0] = tr_read<0>(drd[0] + so);
    SCHED();
    MF(acc[1][0], df, hc[1]);
    if (RD) dhn[0] = tr_read<1024>(drd[0] + so);
    SCHED();
    MF(acc[2][0], df, hc[2]);
    if (RD) hn[0] = ld128<0>(hra + so);
    SCHED();
    MF(acc[3][0], df, hc[3]);
    if (RD) hn[1] = ld128<1024>(hra + so);
    SCHED();
    df = cat8(dl[1], dh[1]);
    MF(acc[0][1], df, hc[0]);
    if (RD) dln[1] = tr_read<0>(drd[1] + so);
    SCHED();
    MF(acc[1][1], df, hc[1]);
    if (RD) dhn[1] = tr_read<1024>(drd[1] + so);
    SCHED();
    MF(acc[2][1], df, hc[2]);
    if (RD) hn[2] = ld128<2048>(hra + so);
    SCHED();
    MF(acc[3][1], df, hc[3]);
    if (RD) hn[3] = ld128<3072>(hra + so);
    SCHED();
    df = cat8(dl[2], dh[2]);
    MF(acc[0][2], df, hc[0]);
    if (RD) dln[2] = tr_read<0>(drd[2] + so);
    SCHED();
    MF(acc[1][2], df, hc[1]);
    if (RD) dhn[2] = tr_read<1024>(drd[2] + so);
    SCHED();
    MF(acc[2][2], df, hc[2]);
    if (RD) dln[3] = tr_read<0>(drd[3] + so);
    SCHED();
    MF(acc[3][2], df, hc[3]);
    if (RD) dhn[3] = tr_read<1024>(drd[3] + so);
    SCHED();
    df = cat8(dl[3], dh[3]);
    MF(acc[0][3], df, hc[0]);
    if (LL) glds16(hb + (size_t)S * (128 * 1024) + voffH, l + r * 2048);
    SCHED();
    MF(acc[1][3], df, hc[1]);
    if (LL) glds16(hb + (size_t)S * (128 * 1024) + 1024 + voffH, l + r * 2048 + 1024);
    SCHED();
    MF(acc[2][3], df, hc[2]);
    if (LL) glds16(db + (size_t)S * (32 * D_MODEL * 2) + voffD, l + 8192 + r * 2048);
    SCHED();
    MF(acc[3][3], df, hc[3]);
    if (LL) glds16(db + (size_t)S * (32 * D_MODEL * 2) + 16 * D_MODEL * 2 + voffD, l + 8192 + r * 2048 + 1024);
    SCHED();
    rs = (rs == 4) ? 0 : rs + 1;
    ws = (ws == 4) ? 0 : ws + 1;
  };
#define G2_STEP_A(i, a, b, c) step(i, IC<a>{}, IC<b>{}, IC<c>{}, ha, hb2, dla, dha, dlb, dhb)
#define G2_STEP_B(i, a, b, c) step(i, IC<a>{}, IC<b>{}, IC<c>{}, hb2, ha, dlb, dhb, dla, dha)
  for (int i = 0; i < 22; i += 2) {
    G2_STEP_A(i, 1, 12, 1);
    G2_STEP_B(i + 1, 1, 12, 1);
  }
  G2_STEP_A(22, 1, 12, 1);
  G2_STEP_B(23, 0, 12, 1);
  G2_STEP_A(24, 0, 8, 1);
  G2_STEP_B(25, 0, 4, 1);
  G2_STEP_A(26, 0, 0, 1);
  G2_STEP_B(27, 0, 0, 0);
  BAR();
  STAMP_NOW();

  f32x4 k0[4], k1[4];
  {
    lds_u8* dst = lds + ((1 - H) * 4 + r) * 8192 + lane * 16;
    if (H) {
#pragma unroll
      for (int u = 0; u < 4; ++u) {
        *(LAS f32x4*)(dst + u * 1024) = acc[0][u];
        *(LAS f32x4*)(dst + (4 + u) * 1024) = acc[1][u];
        k0[u] = acc[2][u]; k1[u] = acc[3][u];
      }
    } else {
#pragma unroll
      for (int u = 0; u < 4; ++u) {
        *(LAS f32x4*)(dst + u * 1024) = acc[2][u];
        *(LAS f32x4*)(dst + (4 + u) * 1024) = acc[3][u];
        k0[u] = acc[0][u]; k1[u] = acc[1][u];
      }
    }
  }
  __syncthreads();
  f32x4 keep[4], send[4];
  {
    lds_u8* src = lds + wid * 8192 + lane * 16;
#pragma unroll
    for (int u = 0; u < 4; ++u) {
      k0[u] += *(LAS f32x4*)(src + u * 1024);
      k1[u] += *(LAS f32x4*)(src + (4 + u) * 1024);
    }
    if (kh) {
#pragma unroll
      for (int u = 0; u < 4; ++u) { keep[u] = k1[u]; send[u] = k0[u]; }
    } else {
#pragma unroll
      for (int u = 0; u < 4; ++u) { keep[u] = k0[u]; send[u] = k1[u]; }
    }
  }
  {
    const int pb = bid ^ 2;
    auto rs_x = __builtin_amdgcn_make_buffer_rsrc(hbuf + WS_XCH, 0, 256 * 32768, 0x00020000);
    const unsigned so = (unsigned)(pb * 32768 + wid * 4096 + lane * 16);
#pragma unroll
    for (int u = 0; u < 4; ++u) __builtin_amdgcn_raw_buffer_store_b128(__builtin_bit_cast(u32x4, send[u]), rs_x, so + u * 1024, 0, 16);
    WAIT_V(0);
    BAR();
    if (wid == 0) {
      unsigned* fl_out = (unsigned*)(hbuf + WS_FLAG + (size_t)(pb * 8) * 128);
      unsigned* fl_in = (unsigned*)(hbuf + WS_FLAG + (size_t)(bid * 8) * 128);
      if (lane == 0) __hip_atomic_store(fl_out, 1u, __ATOMIC_RELAXED, __HIP_MEMORY_SCOPE_AGENT);
      while (__builtin_amdgcn_readfirstlane(__hip_atomic_load(fl_in, __ATOMIC_RELAXED, __HIP_MEMORY_SCOPE_AGENT)) == 0u) __builtin_amdgcn_s_sleep(1);
    }
    asm volatile("" ::: "memory");
    BAR();
    asm volatile("" ::: "memory");
    const unsigned si = (unsigned)(bid * 32768 + wid * 4096 + lane * 16);
#pragma unroll
    for (int u = 0; u < 4; ++u) keep[u] += __builtin_bit_cast(f32x4, __builtin_amdgcn_raw_buffer_load_b128(rs_x, si + u * 1024, 0, 16));
  }
  unsigned char* op = out + ((size_t)(mt * 128 + wm * 64 + (2 * H + kh) * 16 + tok) * D_MODEL + nt * 128 + wn * 64) * 2;
#pragma unroll
  for (int u = 0; u < 4; u += 2) {
    unsigned a0 = cvt_pk_bf16(keep[u][0], keep[u][1]), a1 = cvt_pk_bf16(keep[u][2], keep[u][3]);
    unsigned b0 = cvt_pk_bf16(keep[u + 1][0], keep[u + 1][1]), b1 = cvt_pk_bf16(keep[u + 1][2], keep[u + 1][3]);
    auto s0 = __builtin_amdgcn_permlane16_swap(a0, b0, false, false);
    auto s1 = __builtin_amdgcn_permlane16_swap(a1, b1, false, false);
    u32x4 v = {s0[0], s1[0], s0[1], s1[1]};
    const int col = (g & 1) ? (16 * (u + 1) + 4 * (g - 1)) : (16 * u + 4 * g);
    *(u32x4*)(op + col * 2) = v;
  }
  STAMP_NOW();
  STAMP_NOW();
  STAMP_OUT(hbuf + (65u << 20));
}

extern "C" void kernel_launch(void* const* d_in, const int* in_sizes, int n_in, void* d_out, int out_size, void* d_ws,
                              size_t ws_size, hipStream_t stream) {
  const unsigned char* x = (const unsigned char*)d_in[0];
  const unsigned char* wg = (const unsigned char*)d_in[1];
  const unsigned char* wu = (const unsigned char*)d_in[2];
  const unsigned char* wd = (const unsigned char*)d_in[3];
  unsigned char* hbuf = (unsigned char*)d_ws;
  (void)hipFuncSetAttribute((const void*)k_gemm1, hipFuncAttributeMaxDynamicSharedMemorySize, G1_LDS);
  (void)hipFuncSetAttribute((const void*)k_gemm2, hipFuncAttributeMaxDynamicSharedMemorySize, G2_LDS);
  k_gemm1<<<256, 512, G1_LDS, stream>>>(x, wg, wu, hbuf, wd);
  k_gemm2<<<256, 512, G2_LDS, stream>>>(hbuf, wd, (unsigned char*)d_out);
}
```

```cpp
#include <hip/hip_runtime.h>
#include <hip/hip_bf16.h>
#include <stdint.h>
#define PF3 1

typedef short bf16x8 __attribute__((ext_vector_type(8)));
typedef short bf16x4 __attribute__((ext_vector_type(4)));
typedef float f32x4 __attribute__((ext_vector_type(4)));
typedef float f32x2 __attribute__((ext_vector_type(2)));
typedef unsigned u32x2 __attribute__((ext_vector_type(2)));
typedef unsigned u32x4 __attribute__((ext_vector_type(4)));
#define LAS __attribute__((address_space(3)))
typedef LAS unsigned char lds_u8;

constexpr int M_TOK = 2048, D_MODEL = 1024, F_FF = 3584;
constexpr size_t WS_XCH = 16u << 20;
constexpr size_t WS_FLAG = 32u << 20;

#define WAIT_V(n) asm volatile("s_waitcnt vmcnt(%0)" ::"n"(n) : "memory")
#define WAIT_L0() asm volatile("s_waitcnt lgkmcnt(0)" ::: "memory")
#define BAR() __builtin_amdgcn_s_barrier()
#define SCHED() __builtin_amdgcn_sched_barrier(0)
#define FINL __device__ __forceinline__

#define STAMP_DECL
#define STAMP_NOW()
#define STAMP_OUT(base)
template <int N> struct IC { static constexpr int value = N; };

FINL void glds16c(const void* g, lds_u8* l) {
  __builtin_amdgcn_global_load_lds((const unsigned*)g, (LAS unsigned*)l, 16, 0, 16);
}
FINL void glds16(const void* g, lds_u8* l) {
  __builtin_amdgcn_global_load_lds((const unsigned*)g, (LAS unsigned*)l, 16, 0, 0);
}
template <int OFF> FINL bf16x4 tr_read(unsigned addr) {
  bf16x4 r;
  asm volatile("ds_read_b64_tr_b16 %0, %1 offset:%2" : "=v"(r) : "v"(addr), "n"(OFF) : "memory");
  return r;
}
template <int OFF> FINL bf16x8 ld128(unsigned addr) {
  bf16x8 r;
  asm volatile("ds_read_b128 %0, %1 offset:%2" : "=v"(r) : "v"(addr), "n"(OFF) : "memory");
  return r;
}
#define WAIT_L(n) asm volatile("s_waitcnt lgkmcnt(%0)" ::"n"(n) : "memory")
FINL bf16x8 cat8(bf16x4 a, bf16x4 b) { return __builtin_shufflevector(a, b, 0, 1, 2, 3, 4, 5, 6, 7); }
FINL unsigned cvt_pk_bf16(float lo, float hi) {
  unsigned r;
  asm("v_cvt_pk_bf16_f32 %0, %1, %2" : "=v"(r) : "v"(lo), "v"(hi));
  return r;
}
FINL f32x2 geglu2(f32x2 g, f32x2 u) {
  const float c0 = -2.0f * 0.7978845608028654f * 1.4426950408889634f;
  const float c1 = c0 * 0.044715f;
  f32x2 t = g * g;
  f32x2 p = t * c1 + c0;
  f32x2 a = g * p;
  f32x2 e;
  e.x = __builtin_amdgcn_exp2f(a.x);
  e.y = __builtin_amdgcn_exp2f(a.y);
  f32x2 d = e + 1.0f;
  f32x2 r;
  r.x = __builtin_amdgcn_rcpf(d.x);
  r.y = __builtin_amdgcn_rcpf(d.y);
  return (g * r) * u;
}
FINL u32x2 geglu4(f32x4 g, f32x4 u) {
  f32x2 a = geglu2((f32x2){g[0], g[1]}, (f32x2){u[0], u[1]});
  f32x2 b = geglu2((f32x2){g[2], g[3]}, (f32x2){u[2], u[3]});
  u32x2 o;
  o.x = cvt_pk_bf16(a.x, a.y);
  o.y = cvt_pk_bf16(b.x, b.y);
  return o;
}

constexpr int G1_WGRP = 2048;
constexpr int G1_WSLOT = 8 * G1_WGRP;
constexpr int G1_NW = 4;
constexpr int G1_XSLOT = 32768;
constexpr int G1_NX = 3;
constexpr int G1_XOFF = G1_NW * G1_WSLOT;
constexpr int G1_LDS = G1_XOFF + G1_NX * G1_XSLOT;

constexpr int G2_STAGE = 16384;
constexpr unsigned long long MAGIC = 0x5E1F0A3C97B2D461ull;
typedef __attribute__((address_space(1))) unsigned long long gu64;

__global__ __launch_bounds__(512, 2) void k_fused(const unsigned char* __restrict__ x, const unsigned char* __restrict__ wg,
                                                  const unsigned char* __restrict__ wu, unsigned char* hbuf,
                                                  const unsigned char* __restrict__ wd, unsigned char* __restrict__ out) {
  extern __shared__ __attribute__((aligned(16))) unsigned char smem_raw[];
  lds_u8* lds = (lds_u8*)smem_raw;
  const int tid = threadIdx.x;
  const int wid = __builtin_amdgcn_readfirstlane(tid >> 6);
  const int lane = tid & 63;
  const int half = wid >> 2, wm = wid & 3, wn = wid >> 2;
  STAMP_DECL
  STAMP_NOW();
  const int bid = blockIdx.x;
  const int xcd = bid & 7, idx = bid >> 3;
  const int mt = (xcd >> 2) * 4 + (idx & 3);
  const int nt = (xcd & 3) * 8 + (idx >> 2);
  const int g = lane >> 4, q = (lane >> 2) & 3, p = lane & 3, tok = lane & 15;

  int li = lane - ((wid >> 1) & 1) * 8;
  li = (li < 0 || li >= 56) ? 0 : li;
  const unsigned voffW = (unsigned)((4 * wid + li / 14) * (F_FF * 2) + (li % 14) * 16);
  unsigned voffX[2];
#pragma unroll
  for (int i = 0; i < 2; ++i)
    voffX[i] = (unsigned)((8 * (2 * wid + i) + (lane >> 3)) * (D_MODEL * 2) + (((lane & 7) ^ ((i << 2) | (lane >> 4))) * 16));
  const unsigned char* wgb = wg + (size_t)nt * 224;
  const unsigned char* wub = wu + (size_t)nt * 224;
  const unsigned char* xb = x + (size_t)mt * 256 * (D_MODEL * 2);
  const int wdst = wid * G1_WGRP;
  const int xdst = G1_XOFF + wid * 2048;

  const int rot2 = 0;
  auto stageW = [&](int j, int slot) __attribute__((always_inline)) {
    const size_t go = (size_t)((j + 2 * rot2) & 31) * (32 * F_FF * 2);
    glds16(wgb + go + voffW, lds + slot * G1_WSLOT + wdst);
    glds16(wub + go + voffW, lds + slot * G1_WSLOT + wdst + 1024);
  };
  auto stageX = [&](int t, int hb, int slot) __attribute__((always_inline)) {
    const size_t go = (size_t)hb * (128 * D_MODEL * 2) + (size_t)((t + rot2) & 15) * 128;
    glds16(xb + go + voffX[0], lds + xdst + slot * G1_XSLOT + hb * 16384);
    glds16(xb + go + voffX[1], lds + xdst + slot * G1_XSLOT + hb * 16384 + 1024);
  };

  const unsigned lbase = (unsigned)(size_t)lds;
  const unsigned wrd = lbase + 2 * g * G1_WGRP + (g & 1) * 128 + q * 224 + p * 8 + wn * 128;
  const unsigned wrdm = lbase + 2 * g * G1_WGRP + (g & 1) * 128 + q * 224 + 96 + wn * 16 + (p & 1) * 8 + (p >> 1) * 1024;
  int xrd[2];
#pragma unroll
  for (int s = 0; s < 2; ++s) xrd[s] = G1_XOFF + (wm * 64 + tok) * 128 + ((((s << 2) | g) ^ ((tok >> 1) & 7)) * 16);

  f32x4 accg[4][3], accu[4][3], accm[4];
#pragma unroll
  for (int t = 0; t < 4; ++t) {
#pragma unroll
    for (int u = 0; u < 3; ++u) { accg[t][u] = (f32x4){0.f, 0.f, 0.f, 0.f}; accu[t][u] = (f32x4){0.f, 0.f, 0.f, 0.f}; }
    accm[t] = (f32x4){0.f, 0.f, 0.f, 0.f};
  }

  const int H = wid >> 2, r = wid & 3;
  const int kh = (xcd >> 1) & 1;
  const int mt2 = (xcd >> 2) * 8 + (idx & 7);
  const int nt2 = (xcd & 1) * 4 + (idx >> 3);
  const unsigned char* hb = hbuf + ((size_t)kh * 56 * 128 + mt2 * 8 + 2 * r) * 1024;
  const unsigned char* db = wd + (size_t)kh * 56 * 32 * (D_MODEL * 2) + (size_t)nt2 * 256;
  stageX(0, 0, 0); stageX(0, 1, 0); stageW(0, 0);
  stageW(1, 1); stageX(1, 0, 1);
  stageW(2, 2); stageX(1, 1, 1);
  stageW(3, 3); stageX(2, 0, 2);
  WAIT_V(12);
  BAR();
  STAMP_NOW();

  const unsigned xra[2] = {lbase + (unsigned)xrd[0], lbase + (unsigned)xrd[1]};
  bf16x4 wl[7], wh[7];
  bf16x8 xa[4], xb2[4];
  {
    wl[0] = tr_read<0>(wrd); wh[0] = tr_read<G1_WGRP>(wrd);
    wl[1] = tr_read<32>(wrd); wh[1] = tr_read<32 + G1_WGRP>(wrd);
    wl[2] = tr_read<64>(wrd); wh[2] = tr_read<64 + G1_WGRP>(wrd);
    wl[3] = tr_read<1024>(wrd); wh[3] = tr_read<1024 + G1_WGRP>(wrd);
    wl[4] = tr_read<1024 + 32>(wrd); wh[4] = tr_read<1024 + 32 + G1_WGRP>(wrd);
    wl[5] = tr_read<1024 + 64>(wrd); wh[5] = tr_read<1024 + 64 + G1_WGRP>(wrd);
    wl[6] = tr_read<0>(wrdm); wh[6] = tr_read<G1_WGRP>(wrdm);
    xa[0] = ld128<0>(xra[0]); xa[1] = ld128<2048>(xra[0]); xa[2] = ld128<4096>(xra[0]); xa[3] = ld128<6144>(xra[0]);
    WAIT_L(0);
  }
  int xs_rd = 0, xs_wr = 2;
  auto step = [&](int k, auto S_, auto IW_, auto IX_, auto VM_, auto RD_, bf16x8(&xc)[4], bf16x8(&xn)[4], auto IP_) __attribute__((always_inline)) {
    constexpr int S = decltype(S_)::value;
    constexpr int IP = decltype(IP_)::value;
    constexpr int IW = decltype(IW_)::value, IX = decltype(IX_)::value, VM = decltype(VM_)::value, RD = decltype(RD_)::value;
    if (S) xs_rd = (xs_rd == 2) ? 0 : xs_rd + 1;
    const unsigned wb = ((k + 1) & 3) * G1_WSLOT + wrd;
    const unsigned wbm = ((k + 1) & 3) * G1_WSLOT + wrdm;
    const unsigned xbp = xs_rd * G1_XSLOT + xra[S ^ 1];
#define MF(ACC, WF, XF) ACC = __builtin_amdgcn_mfma_f32_16x16x32_bf16(WF, XF, ACC, 0, 0, 0); SCHED();
    constexpr int LW = IW, LX = IX;
    const size_t goW = (size_t)((k + 4 + 2 * rot2) & 31) * (32 * F_FF * 2);
    const size_t goX = (size_t)(S ^ 1) * (128 * D_MODEL * 2) + (size_t)((((k + 1) >> 1) + 2 + rot2) & 15) * 128;
    lds_u8* ldW = lds + (k & 3) * G1_WSLOT + wdst;
    lds_u8* ldX = lds + xdst + xs_wr * G1_XSLOT + (S ^ 1) * 16384;
    bf16x8 wf;
    WAIT_L(10);
    SCHED();
    wf = cat8(wl[0], wh[0]);
    MF(accg[0][0], wf, xc[0]);
    MF(accg[1][0], wf, xc[1]);
    MF(accg[2][0], wf, xc[2]);
    MF(accg[3][0], wf, xc[3]);
    WAIT_L(0);
    if (RD) {
      if (VM >= 0) WAIT_V(VM);
      BAR();
    }
    SCHED();
    wf = cat8(wl[3], wh[3]);
    MF(accu[0][0], wf, xc[0]);
    if (RD) wl[0] = tr_read<0>(wb);
    SCHED();
    MF(accu[1][0], wf, xc[1]);
    if (RD) wh[0] = tr_read<0 + G1_WGRP>(wb);
    SCHED();
    MF(accu[2][0], wf, xc[2]);
    if (RD) xn[0] = ld128<0>(xbp);
    SCHED();
    MF(accu[3][0], wf, xc[3]);
    if (RD) xn[1] = ld128<2048>(xbp);
    SCHED();
    wf = cat8(wl[1], wh[1]);
    MF(accg[0][1], wf, xc[0]);
    if (RD) wl[3] = tr_read<1024>(wb);
    SCHED();
    MF(accg[1][1], wf, xc[1]);
    if (RD) wh[3] = tr_read<1024 + G1_WGRP>(wb);
    SCHED();
    MF(accg[2][1], wf, xc[2]);
    if (RD) xn[2] = ld128<4096>(xbp);
    SCHED();
    MF(accg[3][1], wf, xc[3]);
    if (RD) xn[3] = ld128<6144>(xbp);
    SCHED();
    wf = cat8(wl[4], wh[4]);
    MF(accu[0][1], wf, xc[0]);
    if (RD) wl[1] = tr_read<32>(wb);
    SCHED();
    MF(accu[1][1], wf, xc[1]);
    if (RD) wh[1] = tr_read<32 + G1_WGRP>(wb);
    SCHED();
    MF(accu[2][1], wf, xc[2]);
    if (LW) glds16(wgb + goW + voffW, ldW);
    SCHED();
    MF(accu[3][1], wf, xc[3]);
    wf = cat8(wl[2], wh[2]);
    MF(accg[0][2], wf, xc[0]);
    if (RD) wl[4] = tr_read<1024 + 32>(wb);
    SCHED();
    MF(accg[1][2], wf, xc[1]);
    if (RD) wh[4] = tr_read<1024 + 32 + G1_WGRP>(wb);
    SCHED();
    MF(accg[2][2], wf, xc[2]);
    if (LW) glds16(wub + goW + voffW, ldW + 1024);
    SCHED();
    MF(accg[3][2], wf, xc[3]);
    wf = cat8(wl[5], wh[5]);
    MF(accu[0][2], wf, xc[0]);
    if (RD) wl[2] = tr_read<64>(wb);
    SCHED();
    MF(accu[1][2], wf, xc[1]);
    if (RD) wh[2] = tr_read<64 + G1_WGRP>(wb);
    SCHED();
    MF(accu[2][2], wf, xc[2]);
    if (LX) glds16(xb + goX + voffX[0], ldX);
    SCHED();
    MF(accu[3][2], wf, xc[3]);
    wf = cat8(wl[6], wh[6]);
    MF(accm[0], wf, xc[0]);
    if (RD) wl[5] = tr_read<1024 + 64>(wb);
    SCHED();
    MF(accm[1], wf, xc[1]);
    if (RD) wh[5] = tr_read<1024 + 64 + G1_WGRP>(wb);
    SCHED();
    MF(accm[2], wf, xc[2]);
    if (LX) glds16(xb + goX + voffX[1], ldX + 1024);
    SCHED();
    MF(accm[3], wf, xc[3]);
    if (RD) wl[6] = tr_read<0>(wbm);
    if (RD) wh[6] = tr_read<0 + G1_WGRP>(wbm);
    SCHED();
    if (!S) xs_wr = (xs_wr == 2) ? 0 : xs_wr + 1;
  };
  for (int k = 0; k < 26; k += 2) {
    step(k, IC<0>{}, IC<1>{}, IC<1>{}, IC<8>{}, IC<1>{}, xa, xb2, IC<0>{});
    step(k + 1, IC<1>{}, IC<1>{}, IC<1>{}, IC<8>{}, IC<1>{}, xb2, xa, IC<0>{});
  }
  step(26, IC<0>{}, IC<1>{}, IC<1>{}, IC<8>{}, IC<1>{}, xa, xb2, IC<0>{});
  step(27, IC<1>{}, IC<1>{}, IC<0>{}, IC<8>{}, IC<1>{}, xb2, xa, IC<0>{});
  step(28, IC<0>{}, IC<0>{}, IC<0>{}, IC<6>{}, IC<1>{}, xa, xb2, IC<0>{});
  step(29, IC<1>{}, IC<0>{}, IC<0>{}, IC<2>{}, IC<1>{}, xb2, xa, IC<0>{});
  step(30, IC<0>{}, IC<0>{}, IC<0>{}, IC<0>{}, IC<1>{}, xa, xb2, IC<0>{});
  step(31, IC<1>{}, IC<0>{}, IC<0>{}, IC<-1>{}, IC<0>{}, xb2, xa, IC<0>{});
  BAR();
  STAMP_NOW();
  const unsigned voffH = (unsigned)(lane * 16);
  const unsigned voffD = (unsigned)((4 * r + g) * (D_MODEL * 2) + ((((tok >> 1) ^ (g | ((r & 1) << 2))) << 1) | (lane & 1)) * 16);
  auto kmap = [&](int sg) __attribute__((always_inline)) -> int {
    return sg < 32 ? 7 * ((sg & 15) >> 1) + 4 * (sg & 1) + 2 * (sg >> 4) : 7 * ((sg - 32) / 3) + 1 + 2 * ((sg - 32) % 3);
  };
#pragma unroll
  for (int i = 0; i < 5; ++i) {
    const int S = kmap(2 * i + H);
    lds_u8* l = lds + (2 * i + H) * G2_STAGE;
    glds16(db + (size_t)S * (32 * D_MODEL * 2) + voffD, l + 8192 + r * 2048);
    glds16(db + (size_t)S * (32 * D_MODEL * 2) + 16 * D_MODEL * 2 + voffD, l + 8192 + r * 2048 + 1024);
  }
  unsigned pf3;
  {
    const int e2 = (wid < 7 ? wid : 6) * 64 + lane;
    const unsigned char* pw = wd + (size_t)(kh * 1792 + idx * 56 + (e2 >> 3)) * 2048 + (xcd & 1) * 1024 + (e2 & 7) * 128;
    asm volatile("global_load_dword %0, %1, off" : "=v"(pf3) : "v"(pw) : "memory");
  }

  auto rs_h = __builtin_amdgcn_make_buffer_rsrc(hbuf, 0, 112 * 128 * 1024, 0x00020000);
  const int T0 = mt * 16 + wm * 4;
  const unsigned ho = (unsigned)((((nt * 7 + wn * 4) >> 1) * 128 + T0) * 1024 + lane * 16);
  f32x4 sg_[4], su_[4];
  u32x4 hp[4];
  unsigned hpo;
  if (nt & 1) {
    hpo = ho + 128 * 1024;
#pragma unroll
    for (int t = 0; t < 4; ++t) {
      const u32x2 a = geglu4(accg[t][1], accu[t][1]), b = geglu4(accg[t][2], accu[t][2]);
      hp[t] = (u32x4){a.x, a.y, b.x, b.y};
      sg_[t] = accg[t][0]; su_[t] = accu[t][0];
    }
  } else {
    hpo = ho;
#pragma unroll
    for (int t = 0; t < 4; ++t) {
      const u32x2 a = geglu4(accg[t][0], accu[t][0]), b = geglu4(accg[t][1], accu[t][1]);
      hp[t] = (u32x4){a.x, a.y, b.x, b.y};
      sg_[t] = accg[t][2]; su_[t] = accu[t][2];
    }
  }
  if (wn == 0) {
#pragma unroll
    for (int t = 0; t < 4; ++t) __builtin_amdgcn_raw_buffer_store_b128(hp[t], rs_h, hpo + t * 1024, 0, 16);
  }
  u32x2 hs[4], hmx[2];
#pragma unroll
  for (int t = 0; t < 4; ++t) hs[t] = geglu4(sg_[t], su_[t]);
#pragma unroll
  for (int t = 0; t < 4; t += 2) {
    f32x4 gg, uu;
#pragma unroll
    for (int rr = 0; rr < 4; ++rr) {
      auto sw = __builtin_amdgcn_permlane32_swap(__float_as_uint(accm[t][rr]), __float_as_uint(accm[t + 1][rr]), false, false);
      gg[rr] = __uint_as_float(sw[0]);
      uu[rr] = __uint_as_float(sw[1]);
    }
    hmx[t >> 1] = geglu4(gg, uu);
  }
  asm volatile("" : "+v"(hs[0]), "+v"(hs[1]), "+v"(hs[2]), "+v"(hs[3]), "+v"(hmx[0]), "+v"(hmx[1]));
  STAMP_NOW();
  WAIT_V(0);
  asm volatile("" ::"v"(pf3));
  BAR();
  STAMP_NOW();
  gu64* fout;
  gu64* fin = (gu64*)(hbuf + WS_FLAG + (size_t)bid * 512 + (lane & 15) * 8);
  {
    const int cm = 2 * mt + (lane & 1), cn = (lane >> 1) & 7;
    const int cb = (((cm & 7) | ((cn & 3) << 3)) << 3) | ((cm >> 3) << 2) | ((nt >> 4) << 1) | (cn >> 2);
    fout = (gu64*)(hbuf + WS_FLAG + (size_t)cb * 512 + (nt & 15) * 8);
  }
  auto wait_flags = [&](gu64* f) __attribute__((always_inline)) {
    const unsigned long long t0 = __builtin_amdgcn_s_memrealtime();
    for (;;) {
      const bool ok = __hip_atomic_load(f, __ATOMIC_RELAXED, __HIP_MEMORY_SCOPE_AGENT) == MAGIC;
      if (__all(ok)) break;
      if (__builtin_amdgcn_s_memrealtime() - t0 > 5000000ull) break;
      __builtin_amdgcn_s_sleep(1);
    }
  };
  if (wid == 0) {
    if (lane < 16) __hip_atomic_store(fout, MAGIC, __ATOMIC_RELAXED, __HIP_MEMORY_SCOPE_AGENT);
    wait_flags(fin);
    __builtin_amdgcn_s_waitcnt(0xC07F);
  }
  if (wn == 1) {
#pragma unroll
    for (int t = 0; t < 4; ++t) __builtin_amdgcn_raw_buffer_store_b128(hp[t], rs_h, hpo + t * 1024, 0, 16);
  }
  asm volatile("" ::: "memory");
  BAR();
  asm volatile("" ::: "memory");
  STAMP_NOW();

#pragma unroll
  for (int i = 0; i < 5; ++i) {
    const int S = kmap(2 * i + H);
    lds_u8* l = lds + (2 * i + H) * G2_STAGE;
    glds16c(hb + (size_t)S * (128 * 1024) + voffH, l + r * 2048);
    glds16c(hb + (size_t)S * (128 * 1024) + 1024 + voffH, l + r * 2048 + 1024);
  }
  const unsigned hso = (nt & 1) ? ho + 8 : ho + 128 * 1024;
  const unsigned hm = (unsigned)((((nt * 7 + 3) >> 1) * 128 + T0 + (lane >> 5)) * 1024 + ((2 * wn + (g & 1)) * 16 + tok) * 16 + ((nt + 1) & 1) * 8);
  const int wm2 = r & 1, wn2 = r >> 1;
  const int hrd = wm2 * 4096 + lane * 16;
  unsigned drd[4];
  {
    const int f = q | ((g & 1) << 2);
#pragma unroll
    for (int u = 0; u < 4; ++u) drd[u] = lbase + H * G2_STAGE + 8192 + (8 * g + q) * 256 + (((4 * wn2 + u) ^ f) * 32) + 8 * p;
  }
  f32x4 acc[4][4];
#pragma unroll
  for (int t = 0; t < 4; ++t)
#pragma unroll
    for (int u = 0; u < 4; ++u) acc[t][u] = (f32x4){0.f, 0.f, 0.f, 0.f};
  const unsigned hra = lbase + H * G2_STAGE + hrd;
  WAIT_V(8);
  BAR();
  if (wid == 0 && lane < 16) {
    __hip_atomic_store(fout + 16, MAGIC, __ATOMIC_RELAXED, __HIP_MEMORY_SCOPE_AGENT);
    __hip_atomic_store(fin, 0ull, __ATOMIC_RELAXED, __HIP_MEMORY_SCOPE_AGENT);
  }
  STAMP_NOW();
  bf16x4 dla[4], dha[4], dlb[4], dhb[4];
  bf16x8 ha[4], hb2[4];
  {
#pragma unroll
    for (int u = 0; u < 4; ++u) { dla[u] = tr_read<0>(drd[u]); dha[u] = tr_read<1024>(drd[u]); }
    ha[0] = ld128<0>(hra); ha[1] = ld128<1024>(hra); ha[2] = ld128<2048>(hra); ha[3] = ld128<3072>(hra);
  }
  int rs = 1, ws = 0;
  auto step2 = [&](int i, auto IL_, auto VM_, auto RD_, bf16x8(&hc)[4], bf16x8(&hn)[4], bf16x4(&dl)[4], bf16x4(&dh)[4], bf16x4(&dln)[4],
                  bf16x4(&dhn)[4]) __attribute__((always_inline)) {
    constexpr int IL = decltype(IL_)::value, VM = decltype(VM_)::value, RD = decltype(RD_)::value;
    const unsigned so = rs * (2 * G2_STAGE);
    const int S = kmap(2 * (i + 5) + H);
    lds_u8* l = lds + ws * (2 * G2_STAGE) + H * G2_STAGE;
    constexpr int LL = IL;
    bf16x8 df;
    WAIT_L(0);
    if (RD) {
      WAIT_V(VM);
      BAR();
    }
    SCHED();
    df = cat8(dl[0], dh[0]);
    MF(acc[0][0], df, hc[0]);
    if (RD) dln[0] = tr_read<0>(drd[0] + so);
    SCHED();
    MF(acc[1][0], df, hc[1]);
    if (RD) dhn[0] = tr_read<1024>(drd[0] + so);
    SCHED();
    MF(acc[2][0], df, hc[2]);
    if (RD) hn[0] = ld128<0>(hra + so);
    SCHED();
    MF(acc[3][0], df, hc[3]);
    if (RD) hn[1] = ld128<1024>(hra + so);
    SCHED();
    df = cat8(dl[1], dh[1]);
    MF(acc[0][1], df, hc[0]);
    if (RD) dln[1] = tr_read<0>(drd[1] + so);
    SCHED();
    MF(acc[1][1], df, hc[1]);
    if (RD) dhn[1] = tr_read<1024>(drd[1] + so);
    SCHED();
    MF(acc[2][1], df, hc[2]);
    if (RD) hn[2] = ld128<2048>(hra + so);
    SCHED();
    MF(acc[3][1], df, hc[3]);
    if (RD) hn[3] = ld128<3072>(hra + so);
    SCHED();
    df = cat8(dl[2], dh[2]);
    MF(acc[0][2], df, hc[0]);
    if (RD) dln[2] = tr_read<0>(drd[2] + so);
    SCHED();
    MF(acc[1][2], df, hc[1]);
    if (RD) dhn[2] = tr_read<1024>(drd[2] + so);
    SCHED();
    MF(acc[2][2], df, hc[2]);
    if (RD) dln[3] = tr_read<0>(drd[3] + so);
    SCHED();
    MF(acc[3][2], df, hc[3]);
    if (RD) dhn[3] = tr_read<1024>(drd[3] + so);
    SCHED();
    df = cat8(dl[3], dh[3]);
    MF(acc[0][3], df, hc[0]);
    if (LL) glds16c(hb + (size_t)S * (128 * 1024) + voffH, l + r * 2048);
    SCHED();
    MF(acc[1][3], df, hc[1]);
    if (LL) glds16c(hb + (size_t)S * (128 * 1024) + 1024 + voffH, l + r * 2048 + 1024);
    SCHED();
    MF(acc[2][3], df, hc[2]);
    if (LL) glds16(db + (size_t)S * (32 * D_MODEL * 2) + voffD, l + 8192 + r * 2048);
    SCHED();
    MF(acc[3][3], df, hc[3]);
    if (LL) glds16(db + (size_t)S * (32 * D_MODEL * 2) + 16 * D_MODEL * 2 + voffD, l + 8192 + r * 2048 + 1024);
    SCHED();
    rs = (rs == 4) ? 0 : rs + 1;
    ws = (ws == 4) ? 0 : ws + 1;
  };
#define G2_STEP_A(i, a, b, c) step2(i, IC<a>{}, IC<b>{}, IC<c>{}, ha, hb2, dla, dha, dlb, dhb)
#define G2_STEP_B(i, a, b, c) step2(i, IC<a>{}, IC<b>{}, IC<c>{}, hb2, ha, dlb, dhb, dla, dha)
  unsigned long long pv;
  auto poll_issue = [&](gu64* f) __attribute__((always_inline)) {
    asm volatile("global_load_dwordx2 %0, %1, off sc1" : "=v"(pv) : "v"(f) : "memory");
  };
  auto poll_check = [&](gu64* f) __attribute__((always_inline)) {
    asm volatile("" : "+v"(pv));
    if (!__all(pv == MAGIC)) wait_flags(f);
    if (lane < 16) __hip_atomic_store(f, 0ull, __ATOMIC_RELAXED, __HIP_MEMORY_SCOPE_AGENT);
    __builtin_amdgcn_s_waitcnt(0xC07F);
  };
  __builtin_amdgcn_raw_buffer_store_b64(hs[0], rs_h, hso, 0, 16);
  __builtin_amdgcn_raw_buffer_store_b64(hs[1], rs_h, hso + 1024, 0, 16);
  G2_STEP_A(0, 1, 8, 1);
  __builtin_amdgcn_raw_buffer_store_b64(hs[2], rs_h, hso + 2048, 0, 16);
  __builtin_amdgcn_raw_buffer_store_b64(hs[3], rs_h, hso + 3072, 0, 16);
  G2_STEP_B(1, 1, 12, 1);
  if (wid == 0) poll_issue(fin + 16);
  __builtin_amdgcn_raw_buffer_store_b64(hmx[0], rs_h, hm, 0, 16);
  __builtin_amdgcn_raw_buffer_store_b64(hmx[1], rs_h, hm + 2048, 0, 16);
  G2_STEP_A(2, 1, 16, 1);
  if (wid == 0) {
    WAIT_V(6);
    poll_check(fin + 16);
  }
  asm volatile("" ::: "memory");
  G2_STEP_B(3, 1, 18, 1);
  G2_STEP_A(4, 1, 16, 1);
  G2_STEP_B(5, 1, 14, 1);
  G2_STEP_A(6, 1, 12, 1);
  if (wid == 0 && lane < 16) __hip_atomic_store(fout + 32, MAGIC, __ATOMIC_RELAXED, __HIP_MEMORY_SCOPE_AGENT);
  G2_STEP_B(7, 1, 12, 1);
  G2_STEP_A(8, 1, 12, 1);
  G2_STEP_B(9, 1, 12, 1);
  if (wid == 0) poll_issue(fin + 32);
  G2_STEP_A(10, 1, 12, 1);
  if (wid == 0) {
    WAIT_V(4);
    poll_check(fin + 32);
  }
  asm volatile("" ::: "memory");
  G2_STEP_B(11, 1, 12, 1);
  for (int i = 12; i < 22; i += 2) {
    G2_STEP_A(i, 1, 12, 1);
    G2_STEP_B(i + 1, 1, 12, 1);
  }
  G2_STEP_A(22, 1, 12, 1);
  G2_STEP_B(23, 0, 12, 1);
  G2_STEP_A(24, 0, 8, 1);
  G2_STEP_B(25, 0, 4, 1);
  G2_STEP_A(26, 0, 0, 1);
  G2_STEP_B(27, 0, 0, 0);
  BAR();
  STAMP_NOW();

  f32x4 k0[4], k1[4];
  {
    lds_u8* dst = lds + ((1 - H) * 4 + r) * 8192 + lane * 16;
    if (H) {
#pragma unroll
      for (int u = 0; u < 4; ++u) {
        *(LAS f32x4*)(dst + u * 1024) = acc[0][u];
        *(LAS f32x4*)(dst + (4 + u) * 1024) = acc[1][u];
        k0[u] = acc[2][u]; k1[u] = acc[3][u];
      }
    } else {
#pragma unroll
      for (int u = 0; u < 4; ++u) {
        *(LAS f32x4*)(dst + u * 1024) = acc[2][u];
        *(LAS f32x4*)(dst + (4 + u) * 1024) = acc[3][u];
        k0[u] = acc[0][u]; k1[u] = acc[1][u];
      }
    }
  }
  __syncthreads();
  f32x4 keep[4], send[4];
  {
    lds_u8* src = lds + wid * 8192 + lane * 16;
#pragma unroll
    for (int u = 0; u < 4; ++u) {
      k0[u] += *(LAS f32x4*)(src + u * 1024);
      k1[u] += *(LAS f32x4*)(src + (4 + u) * 1024);
    }
    if (kh) {
#pragma unroll
      for (int u = 0; u < 4; ++u) { keep[u] = k1[u]; send[u] = k0[u]; }
    } else {
#pragma unroll
      for (int u = 0; u < 4; ++u) { keep[u] = k0[u]; send[u] = k1[u]; }
    }
  }
  STAMP_NOW();
  {
    const int pb = bid ^ 2;
    auto rs_x = __builtin_amdgcn_make_buffer_rsrc(hbuf + WS_XCH, 0, 256 * 32768, 0x00020000);
    const unsigned so = (unsigned)(pb * 32768 + wid * 4096 + lane * 16);
#pragma unroll
    for (int u = 0; u < 4; ++u) __builtin_amdgcn_raw_buffer_store_b128(__builtin_bit_cast(u32x4, send[u]), rs_x, so + u * 1024, 0, 16);
    WAIT_V(0);
    BAR();
    STAMP_NOW();
    if (wid == 0) {
      gu64* fl_out = (gu64*)(hbuf + WS_FLAG + (size_t)pb * 512 + 384);
      gu64* fl_in = (gu64*)(hbuf + WS_FLAG + (size_t)bid * 512 + 384);
      if (lane == 0) __hip_atomic_store(fl_out, MAGIC, __ATOMIC_RELAXED, __HIP_MEMORY_SCOPE_AGENT);
      const unsigned long long t0 = __builtin_amdgcn_s_memrealtime();
      for (;;) {
        const bool ok = __hip_atomic_load(fl_in, __ATOMIC_RELAXED, __HIP_MEMORY_SCOPE_AGENT) == MAGIC;
        if (__all(ok)) break;
        if (__builtin_amdgcn_s_memrealtime() - t0 > 5000000ull) break;
        __builtin_amdgcn_s_sleep(1);
      }
      if (lane == 0) __hip_atomic_store(fl_in, 0ull, __ATOMIC_RELAXED, __HIP_MEMORY_SCOPE_AGENT);
    }
    asm volatile("" ::: "memory");
    BAR();
    asm volatile("" ::: "memory");
    const unsigned si = (unsigned)(bid * 32768 + wid * 4096 + lane * 16);
#pragma unroll
    for (int u = 0; u < 4; ++u) keep[u] += __builtin_bit_cast(f32x4, __builtin_amdgcn_raw_buffer_load_b128(rs_x, si + u * 1024, 0, 16));
  }
  unsigned char* op = out + ((size_t)(mt2 * 128 + wm2 * 64 + (2 * H + kh) * 16 + tok) * D_MODEL + nt2 * 128 + wn2 * 64) * 2;
#pragma unroll
  for (int u = 0; u < 4; u += 2) {
    unsigned a0 = cvt_pk_bf16(keep[u][0], keep[u][1]), a1 = cvt_pk_bf16(keep[u][2], keep[u][3]);
    unsigned b0 = cvt_pk_bf16(keep[u + 1][0], keep[u + 1][1]), b1 = cvt_pk_bf16(keep[u + 1][2], keep[u + 1][3]);
    auto s0 = __builtin_amdgcn_permlane16_swap(a0, b0, false, false);
    auto s1 = __builtin_amdgcn_permlane16_swap(a1, b1, false, false);
    u32x4 v = {s0[0], s1[0], s0[1], s1[1]};
    const int col = (g & 1) ? (16 * (u + 1) + 4 * (g - 1)) : (16 * u + 4 * g);
    *(u32x4*)(op + col * 2) = v;
  }
  STAMP_NOW();
  STAMP_NOW();
  STAMP_OUT(hbuf + (64u << 20));
}

extern "C" void kernel_launch(void* const* d_in, const int* in_sizes, int n_in, void* d_out, int out_size, void* d_ws,
                              size_t ws_size, hipStream_t stream) {
  const unsigned char* x = (const unsigned char*)d_in[0];
  const unsigned char* wg = (const unsigned char*)d_in[1];
  const unsigned char* wu = (const unsigned char*)d_in[2];
  const unsigned char* wd = (const unsigned char*)d_in[3];
  unsigned char* hbuf = (unsigned char*)d_ws;
  (void)hipFuncSetAttribute((const void*)k_fused, hipFuncAttributeMaxDynamicSharedMemorySize, G1_LDS);
  k_fused<<<256, 512, G1_LDS, stream>>>(x, wg, wu, hbuf, wd, (unsigned char*)d_out);
}
```

```cpp
#include <hip/hip_runtime.h>
#include <hip/hip_bf16.h>
#include <stdint.h>
#define PF3 1

typedef short bf16x8 __attribute__((ext_vector_type(8)));
typedef short bf16x4 __attribute__((ext_vector_type(4)));
typedef float f32x4 __attribute__((ext_vector_type(4)));
typedef float f32x2 __attribute__((ext_vector_type(2)));
typedef unsigned u32x2 __attribute__((ext_vector_type(2)));
typedef unsigned u32x4 __attribute__((ext_vector_type(4)));
#define LAS __attribute__((address_space(3)))
typedef LAS unsigned char lds_u8;

constexpr int M_TOK = 2048, D_MODEL = 1024, F_FF = 3584;
constexpr size_t WS_XCH = 16u << 20;
constexpr size_t WS_FLAG = 32u << 20;

#define WAIT_V(n) asm volatile("s_waitcnt vmcnt(%0)" ::"n"(n) : "memory")
#define WAIT_L0() asm volatile("s_waitcnt lgkmcnt(0)" ::: "memory")
#define BAR() __builtin_amdgcn_s_barrier()
#define SCHED() __builtin_amdgcn_sched_barrier(0)
#define FINL __device__ __forceinline__

#define STAMP_DECL
#define STAMP_NOW()
#define STAMP_OUT(base)
template <int N> struct IC { static constexpr int value = N; };

FINL void glds16c(const void* g, lds_u8* l) {
  __builtin_amdgcn_global_load_lds((const unsigned*)g, (LAS unsigned*)l, 16, 0, 16);
}
FINL void glds16(const void* g, lds_u8* l) {
  __builtin_amdgcn_global_load_lds((const unsigned*)g, (LAS unsigned*)l, 16, 0, 0);
}
template <int OFF> FINL bf16x4 tr_read(unsigned addr) {
  bf16x4 r;
  asm volatile("ds_read_b64_tr_b16 %0, %1 offset:%2" : "=v"(r) : "v"(addr), "n"(OFF) : "memory");
  return r;
}
template <int OFF> FINL bf16x8 ld128(unsigned addr) {
  bf16x8 r;
  asm volatile("ds_read_b128 %0, %1 offset:%2" : "=v"(r) : "v"(addr), "n"(OFF) : "memory");
  return r;
}
template <int O0> FINL bf16x8 ld2x64(unsigned addr) {
  bf16x8 r;
  asm volatile("ds_read2st64_b64 %0, %1 offset0:%2 offset1:%3" : "=v"(r) : "v"(addr), "n"(O0), "n"(O0 + 1) : "memory");
  return r;
}
#define WAIT_L(n) asm volatile("s_waitcnt lgkmcnt(%0)" ::"n"(n) : "memory")
FINL bf16x8 cat8(bf16x4 a, bf16x4 b) { return __builtin_shufflevector(a, b, 0, 1, 2, 3, 4, 5, 6, 7); }
FINL unsigned cvt_pk_bf16(float lo, float hi) {
  unsigned r;
  asm("v_cvt_pk_bf16_f32 %0, %1, %2" : "=v"(r) : "v"(lo), "v"(hi));
  return r;
}
FINL f32x2 geglu2(f32x2 g, f32x2 u) {
  const float c0 = -2.0f * 0.7978845608028654f * 1.4426950408889634f;
  const float c1 = c0 * 0.044715f;
  f32x2 t = g * g;
  f32x2 p = t * c1 + c0;
  f32x2 a = g * p;
  f32x2 e;
  e.x = __builtin_amdgcn_exp2f(a.x);
  e.y = __builtin_amdgcn_exp2f(a.y);
  f32x2 d = e + 1.0f;
  f32x2 r;
  r.x = __builtin_amdgcn_rcpf(d.x);
  r.y = __builtin_amdgcn_rcpf(d.y);
  return (g * r) * u;
}
FINL u32x2 geglu4(f32x4 g, f32x4 u) {
  f32x2 a = geglu2((f32x2){g[0], g[1]}, (f32x2){u[0], u[1]});
  f32x2 b = geglu2((f32x2){g[2], g[3]}, (f32x2){u[2], u[3]});
  u32x2 o;
  o.x = cvt_pk_bf16(a.x, a.y);
  o.y = cvt_pk_bf16(b.x, b.y);
  return o;
}

constexpr int G1_WGRP = 2048;
constexpr int G1_WSLOT = 8 * G1_WGRP;
constexpr int G1_NW = 4;
constexpr int G1_XSLOT = 32768;
constexpr int G1_NX = 3;
constexpr int G1_XOFF = G1_NW * G1_WSLOT;
constexpr int G1_LDS = G1_XOFF + G1_NX * G1_XSLOT;

constexpr int G2_STAGE = 16384;
constexpr unsigned long long MAGIC = 0x5E1F0A3C97B2D461ull;
typedef __attribute__((address_space(1))) unsigned long long gu64;

__global__ __launch_bounds__(512, 2) void k_fused(const unsigned char* __restrict__ x, const unsigned char* __restrict__ wg,
                                                  const unsigned char* __restrict__ wu, unsigned char* hbuf,
                                                  const unsigned char* __restrict__ wd, unsigned char* __restrict__ out) {
  extern __shared__ __attribute__((aligned(16))) unsigned char smem_raw[];
  lds_u8* lds = (lds_u8*)smem_raw;
  const int tid = threadIdx.x;
  const int wid = __builtin_amdgcn_readfirstlane(tid >> 6);
  const int lane = tid & 63;
  const int half = wid >> 2, wm = wid & 3, wn = wid >> 2;
  STAMP_DECL
  STAMP_NOW();
  const int bid = blockIdx.x;
  const int xcd = bid & 7, idx = bid >> 3;
  const int mt = (xcd >> 2) * 4 + (idx & 3);
  const int nt = (xcd & 3) * 8 + (idx >> 2);
  const int g = lane >> 4, q = (lane >> 2) & 3, p = lane & 3, tok = lane & 15;

  int li = lane - ((wid >> 1) & 1) * 8;
  li = (li < 0 || li >= 56) ? 0 : li;
  const unsigned voffW = (unsigned)((4 * wid + li / 14) * (F_FF * 2) + (li % 14) * 16);
  unsigned voffX[2];
#pragma unroll
  for (int i = 0; i < 2; ++i)
    voffX[i] = (unsigned)((8 * (2 * wid + i) + (lane >> 3)) * (D_MODEL * 2) + (((lane & 7) ^ ((i << 2) | (lane >> 4))) * 16));
  const unsigned char* wgb = wg + (size_t)nt * 224;
  const unsigned char* wub = wu + (size_t)nt * 224;
  const unsigned char* xb = x + (size_t)mt * 256 * (D_MODEL * 2);
  const int wdst = wid * G1_WGRP;
  const int xdst = G1_XOFF + wid * 2048;

  const int rot2 = 0;
  auto stageW = [&](int j, int slot) __attribute__((always_inline)) {
    const size_t go = (size_t)((j + 2 * rot2) & 31) * (32 * F_FF * 2);
    glds16(wgb + go + voffW, lds + slot * G1_WSLOT + wdst);
    glds16(wub + go + voffW, lds + slot * G1_WSLOT + wdst + 1024);
  };
  auto stageX = [&](int t, int hb, int slot) __attribute__((always_inline)) {
    const size_t go = (size_t)hb * (128 * D_MODEL * 2) + (size_t)((t + rot2) & 15) * 128;
    glds16(xb + go + voffX[0], lds + xdst + slot * G1_XSLOT + hb * 16384);
    glds16(xb + go + voffX[1], lds + xdst + slot * G1_XSLOT + hb * 16384 + 1024);
  };

  const unsigned lbase = (unsigned)(size_t)lds;
  const unsigned wrd = lbase + 2 * g * G1_WGRP + (g & 1) * 128 + q * 224 + p * 8 + wn * 128;
  const unsigned wrdm = lbase + 2 * g * G1_WGRP + (g & 1) * 128 + q * 224 + 96 + wn * 16 + (p & 1) * 8 + (p >> 1) * 1024;
  int xrd[2];
#pragma unroll
  for (int s = 0; s < 2; ++s) xrd[s] = G1_XOFF + (wm * 64 + tok) * 128 + ((((s << 2) | g) ^ ((tok >> 1) & 7)) * 16);

  f32x4 accg[4][3], accu[4][3], accm[4];
#pragma unroll
  for (int t = 0; t < 4; ++t) {
#pragma unroll
    for (int u = 0; u < 3; ++u) { accg[t][u] = (f32x4){0.f, 0.f, 0.f, 0.f}; accu[t][u] = (f32x4){0.f, 0.f, 0.f, 0.f}; }
    accm[t] = (f32x4){0.f, 0.f, 0.f, 0.f};
  }

  const int H = wid >> 2, r = wid & 3;
  const int kh = (xcd >> 1) & 1;
  const int mt2 = (xcd >> 2) * 8 + (idx & 7);
  const int nt2 = (xcd & 1) * 4 + (idx >> 3);
  const unsigned char* hb = hbuf + ((size_t)kh * 56 * 128 + mt2 * 8 + 2 * r) * 1024;
  const unsigned char* db = wd + (size_t)kh * 56 * 32 * (D_MODEL * 2) + (size_t)nt2 * 256;
  stageX(0, 0, 0); stageX(0, 1, 0); stageW(0, 0);
  stageW(1, 1); stageX(1, 0, 1);
  stageW(2, 2); stageX(1, 1, 1);
  stageW(3, 3); stageX(2, 0, 2);
  WAIT_V(12);
  BAR();
  STAMP_NOW();

  const unsigned xra[2] = {lbase + (unsigned)xrd[0], lbase + (unsigned)xrd[1]};
  bf16x4 wl[7], wh[7];
  bf16x8 xa[4], xb2[4];
  {
    wl[0] = tr_read<0>(wrd); wh[0] = tr_read<G1_WGRP>(wrd);
    wl[1] = tr_read<32>(wrd); wh[1] = tr_read<32 + G1_WGRP>(wrd);
    wl[2] = tr_read<64>(wrd); wh[2] = tr_read<64 + G1_WGRP>(wrd);
    wl[3] = tr_read<1024>(wrd); wh[3] = tr_read<1024 + G1_WGRP>(wrd);
    wl[4] = tr_read<1024 + 32>(wrd); wh[4] = tr_read<1024 + 32 + G1_WGRP>(wrd);
    wl[5] = tr_read<1024 + 64>(wrd); wh[5] = tr_read<1024 + 64 + G1_WGRP>(wrd);
    wl[6] = tr_read<0>(wrdm); wh[6] = tr_read<G1_WGRP>(wrdm);
    xa[0] = ld128<0>(xra[0]); xa[1] = ld128<2048>(xra[0]); xa[2] = ld128<4096>(xra[0]); xa[3] = ld128<6144>(xra[0]);
    WAIT_L(0);
  }
  int xs_rd = 0, xs_wr = 2;
  auto step = [&](int k, auto S_, auto IW_, auto IX_, auto VM_, auto RD_, bf16x8(&xc)[4], bf16x8(&xn)[4], auto IP_) __attribute__((always_inline)) {
    constexpr int S = decltype(S_)::value;
    constexpr int IP = decltype(IP_)::value;
    constexpr int IW = decltype(IW_)::value, IX = decltype(IX_)::value, VM = decltype(VM_)::value, RD = decltype(RD_)::value;
    if (S) xs_rd = (xs_rd == 2) ? 0 : xs_rd + 1;
    const unsigned wb = ((k + 1) & 3) * G1_WSLOT + wrd;
    const unsigned wbm = ((k + 1) & 3) * G1_WSLOT + wrdm;
    const unsigned xbp = xs_rd * G1_XSLOT + xra[S ^ 1];
#define MF(ACC, WF, XF) ACC = __builtin_amdgcn_mfma_f32_16x16x32_bf16(WF, XF, ACC, 0, 0, 0); SCHED();
    constexpr int LW = IW, LX = IX;
    const size_t goW = (size_t)((k + 4 + 2 * rot2) & 31) * (32 * F_FF * 2);
    const size_t goX = (size_t)(S ^ 1) * (128 * D_MODEL * 2) + (size_t)((((k + 1) >> 1) + 2 + rot2) & 15) * 128;
    lds_u8* ldW = lds + (k & 3) * G1_WSLOT + wdst;
    lds_u8* ldX = lds + xdst + xs_wr * G1_XSLOT + (S ^ 1) * 16384;
    bf16x8 wf;
    WAIT_L(10);
    SCHED();
    wf = cat8(wl[0], wh[0]);
    MF(accg[0][0], wf, xc[0]);
    MF(accg[1][0], wf, xc[1]);
    MF(accg[2][0], wf, xc[2]);
    MF(accg[3][0], wf, xc[3]);
    WAIT_L(0);
    if (RD) {
      if (VM >= 0) WAIT_V(VM);
      BAR();
    }
    SCHED();
    wf = cat8(wl[3], wh[3]);
    MF(accu[0][0], wf, xc[0]);
    if (RD) wl[0] = tr_read<0>(wb);
    SCHED();
    MF(accu[1][0], wf, xc[1]);
    if (RD) wh[0] = tr_read<0 + G1_WGRP>(wb);
    SCHED();
    MF(accu[2][0], wf, xc[2]);
    if (RD) xn[0] = ld128<0>(xbp);
    SCHED();
    MF(accu[3][0], wf, xc[3]);
    if (RD) xn[1] = ld128<2048>(xbp);
    SCHED();
    wf = cat8(wl[1], wh[1]);
    MF(accg[0][1], wf, xc[0]);
    if (RD) wl[3] = tr_read<1024>(wb);
    SCHED();
    MF(accg[1][1], wf, xc[1]);
    if (RD) wh[3] = tr_read<1024 + G1_WGRP>(wb);
    SCHED();
    MF(accg[2][1], wf, xc[2]);
    if (RD) xn[2] = ld128<4096>(xbp);
    SCHED();
    MF(accg[3][1], wf, xc[3]);
    if (RD) xn[3] = ld128<6144>(xbp);
    SCHED();
    wf = cat8(wl[4], wh[4]);
    MF(accu[0][1], wf, xc[0]);
    if (RD) wl[1] = tr_read<32>(wb);
    SCHED();
    MF(accu[1][1], wf, xc[1]);
    if (RD) wh[1] = tr_read<32 + G1_WGRP>(wb);
    SCHED();
    MF(accu[2][1], wf, xc[2]);
    if (LW) glds16(wgb + goW + voffW, ldW);
    SCHED();
    MF(accu[3][1], wf, xc[3]);
    wf = cat8(wl[2], wh[2]);
    MF(accg[0][2], wf, xc[0]);
    if (RD) wl[4] = tr_read<1024 + 32>(wb);
    SCHED();
    MF(accg[1][2], wf, xc[1]);
    if (RD) wh[4] = tr_read<1024 + 32 + G1_WGRP>(wb);
    SCHED();
    MF(accg[2][2], wf, xc[2]);
    if (LW) glds16(wub + goW + voffW, ldW + 1024);
    SCHED();
    MF(accg[3][2], wf, xc[3]);
    wf = cat8(wl[5], wh[5]);
    MF(accu[0][2], wf, xc[0]);
    if (RD) wl[2] = tr_read<64>(wb);
    SCHED();
    MF(accu[1][2], wf, xc[1]);
    if (RD) wh[2] = tr_read<64 + G1_WGRP>(wb);
    SCHED();
    MF(accu[2][2], wf, xc[2]);
    if (LX) glds16(xb + goX + voffX[0], ldX);
    SCHED();
    MF(accu[3][2], wf, xc[3]);
    wf = cat8(wl[6], wh[6]);
    MF(accm[0], wf, xc[0]);
    if (RD) wl[5] = tr_read<1024 + 64>(wb);
    SCHED();
    MF(accm[1], wf, xc[1]);
    if (RD) wh[5] = tr_read<1024 + 64 + G1_WGRP>(wb);
    SCHED();
    MF(accm[2], wf, xc[2]);
    if (LX) glds16(xb + goX + voffX[1], ldX + 1024);
    SCHED();
    MF(accm[3], wf, xc[3]);
    if (RD) wl[6] = tr_read<0>(wbm);
    if (RD) wh[6] = tr_read<0 + G1_WGRP>(wbm);
    SCHED();
    if (!S) xs_wr = (xs_wr == 2) ? 0 : xs_wr + 1;
  };
  for (int k = 0; k < 26; k += 2) {
    step(k, IC<0>{}, IC<1>{}, IC<1>{}, IC<8>{}, IC<1>{}, xa, xb2, IC<0>{});
    step(k + 1, IC<1>{}, IC<1>{}, IC<1>{}, IC<8>{}, IC<1>{}, xb2, xa, IC<0>{});
  }
  step(26, IC<0>{}, IC<1>{}, IC<1>{}, IC<8>{}, IC<1>{}, xa, xb2, IC<0>{});
  step(27, IC<1>{}, IC<1>{}, IC<0>{}, IC<8>{}, IC<1>{}, xb2, xa, IC<0>{});
  {
    bf16x8 wf;
    WAIT_L(0);
    WAIT_V(6);
    BAR();
    SCHED();
    wf = cat8(wl[0], wh[0]);
    MF(accg[0][0], wf, xa[0]);
    xb2[0] = ld128<0>((xra[1] + 2 * G1_XSLOT));
    SCHED();
    MF(accg[1][0], wf, xa[1]);
    xb2[1] = ld128<2048>((xra[1] + 2 * G1_XSLOT));
    SCHED();
    MF(accg[2][0], wf, xa[2]);
    xb2[2] = ld128<4096>((xra[1] + 2 * G1_XSLOT));
    SCHED();
    MF(accg[3][0], wf, xa[3]);
    xb2[3] = ld128<6144>((xra[1] + 2 * G1_XSLOT));
    SCHED();
    wf = cat8(wl[3], wh[3]);
    MF(accu[0][0], wf, xa[0]);
    wl[0] = tr_read<0>((wrd + 1 * G1_WSLOT));
    SCHED();
    MF(accu[1][0], wf, xa[1]);
    wh[0] = tr_read<0 + G1_WGRP>((wrd + 1 * G1_WSLOT));
    SCHED();
    MF(accu[2][0], wf, xa[2]);
    MF(accu[3][0], wf, xa[3]);
    wf = cat8(wl[1], wh[1]);
    MF(accg[0][1], wf, xa[0]);
    wl[3] = tr_read<1024>((wrd + 1 * G1_WSLOT));
    SCHED();
    MF(accg[1][1], wf, xa[1]);
    wh[3] = tr_read<1024 + G1_WGRP>((wrd + 1 * G1_WSLOT));
    SCHED();
    MF(accg[2][1], wf, xa[2]);
    MF(accg[3][1], wf, xa[3]);
    wf = cat8(wl[4], wh[4]);
    MF(accu[0][1], wf, xa[0]);
    wl[1] = tr_read<32>((wrd + 1 * G1_WSLOT));
    SCHED();
    MF(accu[1][1], wf, xa[1]);
    wh[1] = tr_read<32 + G1_WGRP>((wrd + 1 * G1_WSLOT));
    SCHED();
    MF(accu[2][1], wf, xa[2]);
    MF(accu[3][1], wf, xa[3]);
    wl[4] = tr_read<1024 + 32>((wrd + 1 * G1_WSLOT));
    wh[4] = tr_read<1024 + 32 + G1_WGRP>((wrd + 1 * G1_WSLOT));
    SCHED();
  }
  {
    bf16x8 wf;
    WAIT_L(0);
    WAIT_V(2);
    BAR();
    SCHED();
    wf = cat8(wl[0], wh[0]);
    MF(accg[0][0], wf, xb2[0]);
    xa[0] = ld128<0>((xra[0] + 0 * G1_XSLOT));
    SCHED();
    MF(accg[1][0], wf, xb2[1]);
    xa[1] = ld128<2048>((xra[0] + 0 * G1_XSLOT));
    SCHED();
    MF(accg[2][0], wf, xb2[2]);
    xa[2] = ld128<4096>((xra[0] + 0 * G1_XSLOT));
    SCHED();
    MF(accg[3][0], wf, xb2[3]);
    xa[3] = ld128<6144>((xra[0] + 0 * G1_XSLOT));
    SCHED();
    wf = cat8(wl[3], wh[3]);
    MF(accu[0][0], wf, xb2[0]);
    wl[0] = tr_read<0>((wrd + 2 * G1_WSLOT));
    SCHED();
    MF(accu[1][0], wf, xb2[1]);
    wh[0] = tr_read<0 + G1_WGRP>((wrd + 2 * G1_WSLOT));
    SCHED();
    MF(accu[2][0], wf, xb2[2]);
    MF(accu[3][0], wf, xb2[3]);
    wf = cat8(wl[1], wh[1]);
    MF(accg[0][1], wf, xb2[0]);
    wl[3] = tr_read<1024>((wrd + 2 * G1_WSLOT));
    SCHED();
    MF(accg[1][1], wf, xb2[1]);
    wh[3] = tr_read<1024 + G1_WGRP>((wrd + 2 * G1_WSLOT));
    SCHED();
    MF(accg[2][1], wf, xb2[2]);
    MF(accg[3][1], wf, xb2[3]);
    wf = cat8(wl[4], wh[4]);
    MF(accu[0][1], wf, xb2[0]);
    wl[1] = tr_read<32>((wrd + 2 * G1_WSLOT));
    SCHED();
    MF(accu[1][1], wf, xb2[1]);
    wh[1] = tr_read<32 + G1_WGRP>((wrd + 2 * G1_WSLOT));
    SCHED();
    MF(accu[2][1], wf, xb2[2]);
    MF(accu[3][1], wf, xb2[3]);
    wl[4] = tr_read<1024 + 32>((wrd + 2 * G1_WSLOT));
    wh[4] = tr_read<1024 + 32 + G1_WGRP>((wrd + 2 * G1_WSLOT));
    SCHED();
  }
  {
    bf16x8 wf;
    WAIT_L(0);
    WAIT_V(0);
    BAR();
    SCHED();
    wf = cat8(wl[0], wh[0]);
    MF(accg[0][0], wf, xa[0]);
    xb2[0] = ld128<0>((xra[1] + 0 * G1_XSLOT));
    SCHED();
    MF(accg[1][0], wf, xa[1]);
    xb2[1] = ld128<2048>((xra[1] + 0 * G1_XSLOT));
    SCHED();
    MF(accg[2][0], wf, xa[2]);
    xb2[2] = ld128<4096>((xra[1] + 0 * G1_XSLOT));
    SCHED();
    MF(accg[3][0], wf, xa[3]);
    xb2[3] = ld128<6144>((xra[1] + 0 * G1_XSLOT));
    SCHED();
    wf = cat8(wl[3], wh[3]);
    MF(accu[0][0], wf, xa[0]);
    wl[0] = tr_read<0>((wrd + 3 * G1_WSLOT));
    SCHED();
    MF(accu[1][0], wf, xa[1]);
    wh[0] = tr_read<0 + G1_WGRP>((wrd + 3 * G1_WSLOT));
    SCHED();
    MF(accu[2][0], wf, xa[2]);
    MF(accu[3][0], wf, xa[3]);
    wf = cat8(wl[1], wh[1]);
    MF(accg[0][1], wf, xa[0]);
    wl[3] = tr_read<1024>((wrd + 3 * G1_WSLOT));
    SCHED();
    MF(accg[1][1], wf, xa[1]);
    wh[3] = tr_read<1024 + G1_WGRP>((wrd + 3 * G1_WSLOT));
    SCHED();
    MF(accg[2][1], wf, xa[2]);
    MF(accg[3][1], wf, xa[3]);
    wf = cat8(wl[4], wh[4]);
    MF(accu[0][1], wf, xa[0]);
    wl[1] = tr_read<32>((wrd + 3 * G1_WSLOT));
    SCHED();
    MF(accu[1][1], wf, xa[1]);
    wh[1] = tr_read<32 + G1_WGRP>((wrd + 3 * G1_WSLOT));
    SCHED();
    MF(accu[2][1], wf, xa[2]);
    MF(accu[3][1], wf, xa[3]);
    wl[4] = tr_read<1024 + 32>((wrd + 3 * G1_WSLOT));
    wh[4] = tr_read<1024 + 32 + G1_WGRP>((wrd + 3 * G1_WSLOT));
    SCHED();
  }
  {
    bf16x8 wf;
    WAIT_L(0);
    SCHED();
    wf = cat8(wl[0], wh[0]);
    MF(accg[0][0], wf, xb2[0]);
    xa[0] = ld128<0>((xra[0] + 2 * G1_XSLOT));
    SCHED();
    MF(accg[1][0], wf, xb2[1]);
    xa[1] = ld128<2048>((xra[0] + 2 * G1_XSLOT));
    SCHED();
    MF(accg[2][0], wf, xb2[2]);
    xa[2] = ld128<4096>((xra[0] + 2 * G1_XSLOT));
    SCHED();
    MF(accg[3][0], wf, xb2[3]);
    xa[3] = ld128<6144>((xra[0] + 2 * G1_XSLOT));
    SCHED();
    wf = cat8(wl[3], wh[3]);
    MF(accu[0][0], wf, xb2[0]);
    MF(accu[1][0], wf, xb2[1]);
    MF(accu[2][0], wf, xb2[2]);
    MF(accu[3][0], wf, xb2[3]);
    wf = cat8(wl[1], wh[1]);
    MF(accg[0][1], wf, xb2[0]);
    MF(accg[1][1], wf, xb2[1]);
    MF(accg[2][1], wf, xb2[2]);
    MF(accg[3][1], wf, xb2[3]);
    wf = cat8(wl[4], wh[4]);
    MF(accu[0][1], wf, xb2[0]);
    MF(accu[1][1], wf, xb2[1]);
    MF(accu[2][1], wf, xb2[2]);
    MF(accu[3][1], wf, xb2[3]);
    SCHED();
  }
  STAMP_NOW();
  auto rs_h = __builtin_amdgcn_make_buffer_rsrc(hbuf, 0, 112 * 128 * 1024, 0x00020000);
  const int T0 = mt * 16 + wm * 4;
  const int sbase = (nt >> 4) * 56, jj = nt & 15;
  {
    const unsigned ho = (unsigned)(((sbase + 2 * jj + wn) * 128 + T0) * 1024 + lane * 16);
#pragma unroll
    for (int t = 0; t < 4; ++t) {
      const u32x2 a = geglu4(accg[t][0], accu[t][0]), b = geglu4(accg[t][1], accu[t][1]);
      __builtin_amdgcn_raw_buffer_store_b128((u32x4){a.x, a.y, b.x, b.y}, rs_h, ho + t * 1024, 0, 16);
    }
  }
  SCHED();
  STAMP_NOW();
  {
    bf16x8 wf;
    WAIT_L(0);
    SCHED();
    wf = cat8(wl[2], wh[2]);
    MF(accg[0][2], wf, xa[0]);
    xb2[0] = ld128<0>((xra[1] + 2 * G1_XSLOT));
    SCHED();
    MF(accg[1][2], wf, xa[1]);
    xb2[1] = ld128<2048>((xra[1] + 2 * G1_XSLOT));
    SCHED();
    MF(accg[2][2], wf, xa[2]);
    xb2[2] = ld128<4096>((xra[1] + 2 * G1_XSLOT));
    SCHED();
    MF(accg[3][2], wf, xa[3]);
    xb2[3] = ld128<6144>((xra[1] + 2 * G1_XSLOT));
    SCHED();
    wf = cat8(wl[5], wh[5]);
    MF(accu[0][2], wf, xa[0]);
    wl[2] = tr_read<64>((wrd + 1 * G1_WSLOT));
    SCHED();
    MF(accu[1][2], wf, xa[1]);
    wh[2] = tr_read<64 + G1_WGRP>((wrd + 1 * G1_WSLOT));
    SCHED();
    MF(accu[2][2], wf, xa[2]);
    MF(accu[3][2], wf, xa[3]);
    wf = cat8(wl[6], wh[6]);
    MF(accm[0], wf, xa[0]);
    wl[5] = tr_read<1024 + 64>((wrd + 1 * G1_WSLOT));
    SCHED();
    MF(accm[1], wf, xa[1]);
    wh[5] = tr_read<1024 + 64 + G1_WGRP>((wrd + 1 * G1_WSLOT));
    SCHED();
    MF(accm[2], wf, xa[2]);
    MF(accm[3], wf, xa[3]);
    wl[6] = tr_read<0>((wrdm + 1 * G1_WSLOT));
    wh[6] = tr_read<0 + G1_WGRP>((wrdm + 1 * G1_WSLOT));
    SCHED();
  }
  {
    bf16x8 wf;
    WAIT_L(0);
    SCHED();
    wf = cat8(wl[2], wh[2]);
    MF(accg[0][2], wf, xb2[0]);
    xa[0] = ld128<0>((xra[0] + 0 * G1_XSLOT));
    SCHED();
    MF(accg[1][2], wf, xb2[1]);
    xa[1] = ld128<2048>((xra[0] + 0 * G1_XSLOT));
    SCHED();
    MF(accg[2][2], wf, xb2[2]);
    xa[2] = ld128<4096>((xra[0] + 0 * G1_XSLOT));
    SCHED();
    MF(accg[3][2], wf, xb2[3]);
    xa[3] = ld128<6144>((xra[0] + 0 * G1_XSLOT));
    SCHED();
    wf = cat8(wl[5], wh[5]);
    MF(accu[0][2], wf, xb2[0]);
    wl[2] = tr_read<64>((wrd + 2 * G1_WSLOT));
    SCHED();
    MF(accu[1][2], wf, xb2[1]);
    wh[2] = tr_read<64 + G1_WGRP>((wrd + 2 * G1_WSLOT));
    SCHED();
    MF(accu[2][2], wf, xb2[2]);
    MF(accu[3][2], wf, xb2[3]);
    wf = cat8(wl[6], wh[6]);
    MF(accm[0], wf, xb2[0]);
    wl[5] = tr_read<1024 + 64>((wrd + 2 * G1_WSLOT));
    SCHED();
    MF(accm[1], wf, xb2[1]);
    wh[5] = tr_read<1024 + 64 + G1_WGRP>((wrd + 2 * G1_WSLOT));
    SCHED();
    MF(accm[2], wf, xb2[2]);
    MF(accm[3], wf, xb2[3]);
    wl[6] = tr_read<0>((wrdm + 2 * G1_WSLOT));
    wh[6] = tr_read<0 + G1_WGRP>((wrdm + 2 * G1_WSLOT));
    SCHED();
  }
  {
    bf16x8 wf;
    WAIT_L(0);
    SCHED();
    wf = cat8(wl[2], wh[2]);
    MF(accg[0][2], wf, xa[0]);
    xb2[0] = ld128<0>((xra[1] + 0 * G1_XSLOT));
    SCHED();
    MF(accg[1][2], wf, xa[1]);
    xb2[1] = ld128<2048>((xra[1] + 0 * G1_XSLOT));
    SCHED();
    MF(accg[2][2], wf, xa[2]);
    xb2[2] = ld128<4096>((xra[1] + 0 * G1_XSLOT));
    SCHED();
    MF(accg[3][2], wf, xa[3]);
    xb2[3] = ld128<6144>((xra[1] + 0 * G1_XSLOT));
    SCHED();
    wf = cat8(wl[5], wh[5]);
    MF(accu[0][2], wf, xa[0]);
    wl[2] = tr_read<64>((wrd + 3 * G1_WSLOT));
    SCHED();
    MF(accu[1][2], wf, xa[1]);
    wh[2] = tr_read<64 + G1_WGRP>((wrd + 3 * G1_WSLOT));
    SCHED();
    MF(accu[2][2], wf, xa[2]);
    MF(accu[3][2], wf, xa[3]);
    wf = cat8(wl[6], wh[6]);
    MF(accm[0], wf, xa[0]);
    wl[5] = tr_read<1024 + 64>((wrd + 3 * G1_WSLOT));
    SCHED();
    MF(accm[1], wf, xa[1]);
    wh[5] = tr_read<1024 + 64 + G1_WGRP>((wrd + 3 * G1_WSLOT));
    SCHED();
    MF(accm[2], wf, xa[2]);
    MF(accm[3], wf, xa[3]);
    wl[6] = tr_read<0>((wrdm + 3 * G1_WSLOT));
    wh[6] = tr_read<0 + G1_WGRP>((wrdm + 3 * G1_WSLOT));
    SCHED();
  }
  {
    bf16x8 wf;
    WAIT_L(0);
    SCHED();
    wf = cat8(wl[2], wh[2]);
    MF(accg[0][2], wf, xb2[0]);
    MF(accg[1][2], wf, xb2[1]);
    MF(accg[2][2], wf, xb2[2]);
    MF(accg[3][2], wf, xb2[3]);
    wf = cat8(wl[5], wh[5]);
    MF(accu[0][2], wf, xb2[0]);
    MF(accu[1][2], wf, xb2[1]);
    MF(accu[2][2], wf, xb2[2]);
    MF(accu[3][2], wf, xb2[3]);
    wf = cat8(wl[6], wh[6]);
    MF(accm[0], wf, xb2[0]);
    MF(accm[1], wf, xb2[1]);
    MF(accm[2], wf, xb2[2]);
    MF(accm[3], wf, xb2[3]);
    SCHED();
  }
  WAIT_V(0);
  BAR();
  STAMP_NOW();
  const unsigned voffH = (unsigned)(lane * 16);
  const unsigned voffD = (unsigned)((4 * r + g) * (D_MODEL * 2) + ((((tok >> 1) ^ (g | ((r & 1) << 2))) << 1) | (lane & 1)) * 16);
  gu64* fout;
  gu64* fin = (gu64*)(hbuf + WS_FLAG + (size_t)bid * 512 + (lane & 15) * 8);
  {
    const int cm = 2 * mt + (lane & 1), cn = (lane >> 1) & 7;
    const int cb = (((cm & 7) | ((cn & 3) << 3)) << 3) | ((cm >> 3) << 2) | ((nt >> 4) << 1) | (cn >> 2);
    fout = (gu64*)(hbuf + WS_FLAG + (size_t)cb * 512 + (nt & 15) * 8);
  }
  if (wid == 0 && lane < 16) __hip_atomic_store(fout, MAGIC, __ATOMIC_RELAXED, __HIP_MEMORY_SCOPE_AGENT);
  auto wd_t0 = [&](int sg) __attribute__((always_inline)) -> int {
    return sg < 32 ? 7 * (sg >> 1) + 4 * (sg & 1) : sg < 48 ? 7 * (sg - 32) + 2 : 14 * (sg - 48) + 3;
  };
  auto wd_dt = [&](int sg) __attribute__((always_inline)) -> int { return sg < 32 ? 1 : sg < 48 ? 4 : 7; };
#pragma unroll
  for (int i = 0; i < 5; ++i) {
    const int t0 = wd_t0(2 * i + H);
    lds_u8* l = lds + (2 * i + H) * G2_STAGE;
    glds16(db + (size_t)t0 * (16 * D_MODEL * 2) + voffD, l + 8192 + r * 2048);
    glds16(db + (size_t)(t0 + 1) * (16 * D_MODEL * 2) + voffD, l + 8192 + r * 2048 + 1024);
  }
  unsigned pf3;
  {
    const int e2 = (wid < 7 ? wid : 6) * 64 + lane;
    const unsigned char* pw = wd + (size_t)(kh * 1792 + idx * 56 + (e2 >> 3)) * 2048 + (xcd & 1) * 1024 + (e2 & 7) * 128;
    asm volatile("global_load_dword %0, %1, off" : "=v"(pf3) : "v"(pw) : "memory");
  }
  u32x2 hs[4], hmx[2];
#pragma unroll
  for (int t = 0; t < 4; ++t) hs[t] = geglu4(accg[t][2], accu[t][2]);
#pragma unroll
  for (int t = 0; t < 4; t += 2) {
    f32x4 gg, uu;
#pragma unroll
    for (int rr = 0; rr < 4; ++rr) {
      auto sw = __builtin_amdgcn_permlane32_swap(__float_as_uint(accm[t][rr]), __float_as_uint(accm[t + 1][rr]), false, false);
      gg[rr] = __uint_as_float(sw[0]);
      uu[rr] = __uint_as_float(sw[1]);
    }
    hmx[t >> 1] = geglu4(gg, uu);
  }
  asm volatile("" : "+v"(hs[0]), "+v"(hs[1]), "+v"(hs[2]), "+v"(hs[3]), "+v"(hmx[0]), "+v"(hmx[1]));
  STAMP_NOW();
  auto wait_flags = [&](gu64* f) __attribute__((always_inline)) {
    const unsigned long long t0 = __builtin_amdgcn_s_memrealtime();
    for (;;) {
      const bool ok = __hip_atomic_load(f, __ATOMIC_RELAXED, __HIP_MEMORY_SCOPE_AGENT) == MAGIC;
      if (__all(ok)) break;
      if (__builtin_amdgcn_s_memrealtime() - t0 > 5000000ull) break;
      __builtin_amdgcn_s_sleep(1);
    }
  };
  if (wid == 0) {
    wait_flags(fin);
    __builtin_amdgcn_s_waitcnt(0xC07F);
  }
  asm volatile("" ::: "memory");
  BAR();
  asm volatile("" ::: "memory");
  STAMP_NOW();

#pragma unroll
  for (int i = 0; i < 5; ++i) {
    const int S = 2 * i + H;
    lds_u8* l = lds + S * G2_STAGE;
    glds16c(hb + (size_t)S * (128 * 1024) + voffH, l + r * 2048);
    glds16c(hb + (size_t)S * (128 * 1024) + 1024 + voffH, l + r * 2048 + 1024);
  }
  const unsigned hso = (unsigned)(((sbase + 32 + jj) * 128 + T0) * 1024 + wn * 512 + lane * 8);
  const unsigned hm = (unsigned)(((sbase + 48 + (jj >> 1)) * 128 + T0 + (lane >> 5)) * 1024 + (jj & 1) * 512 + ((2 * wn + (g & 1)) * 16 + tok) * 8);
  const int wm2 = r & 1, wn2 = r >> 1;
  const int hrd = wm2 * 4096 + lane * 16;
  unsigned drd[4];
  {
    const int f = q | ((g & 1) << 2);
#pragma unroll
    for (int u = 0; u < 4; ++u) drd[u] = lbase + H * G2_STAGE + 8192 + (8 * g + q) * 256 + (((4 * wn2 + u) ^ f) * 32) + 8 * p;
  }
  f32x4 acc[4][4];
#pragma unroll
  for (int t = 0; t < 4; ++t)
#pragma unroll
    for (int u = 0; u < 4; ++u) acc[t][u] = (f32x4){0.f, 0.f, 0.f, 0.f};
  const unsigned hra = lbase + H * G2_STAGE + hrd;
  const unsigned hra2 = lbase + H * G2_STAGE + wm2 * 4096 + lane * 8;
  WAIT_V(8);
  asm volatile("" ::"v"(pf3));
  BAR();
  if (wid == 0 && lane < 16) __hip_atomic_store(fin, 0ull, __ATOMIC_RELAXED, __HIP_MEMORY_SCOPE_AGENT);
  STAMP_NOW();
  bf16x4 dla[4], dha[4], dlb[4], dhb[4];
  bf16x8 ha[4], hb2[4];
  {
#pragma unroll
    for (int u = 0; u < 4; ++u) { dla[u] = tr_read<0>(drd[u]); dha[u] = tr_read<1024>(drd[u]); }
    ha[0] = ld128<0>(hra); ha[1] = ld128<1024>(hra); ha[2] = ld128<2048>(hra); ha[3] = ld128<3072>(hra);
  }
  int rs = 1, ws = 0;
  auto step2 = [&](int i, auto IL_, auto VM_, auto RD_, bf16x8(&hc)[4], bf16x8(&hn)[4], bf16x4(&dl)[4], bf16x4(&dh)[4], bf16x4(&dln)[4],
                  bf16x4(&dhn)[4]) __attribute__((always_inline)) {
    constexpr int IL = decltype(IL_)::value, VM = decltype(VM_)::value, RD = decltype(RD_)::value;
    const unsigned so = rs * (2 * G2_STAGE);
    const int S = 2 * (i + 5) + H;
    const int t0 = wd_t0(S), t1 = t0 + wd_dt(S);
    lds_u8* l = lds + ws * (2 * G2_STAGE) + H * G2_STAGE;
    constexpr int LL = IL;
    bf16x8 df;
    WAIT_L(0);
    if (RD) {
      WAIT_V(VM);
      BAR();
    }
    SCHED();
    df = cat8(dl[0], dh[0]);
    MF(acc[0][0], df, hc[0]);
    if (RD) dln[0] = tr_read<0>(drd[0] + so);
    SCHED();
    MF(acc[1][0], df, hc[1]);
    if (RD) dhn[0] = tr_read<1024>(drd[0] + so);
    SCHED();
    MF(acc[2][0], df, hc[2]);
    if (RD == 1) hn[0] = ld128<0>(hra + so);
    if (RD == 2) hn[0] = ld2x64<0>(hra2 + so);
    SCHED();
    MF(acc[3][0], df, hc[3]);
    if (RD == 1) hn[1] = ld128<1024>(hra + so);
    if (RD == 2) hn[1] = ld2x64<2>(hra2 + so);
    SCHED();
    df = cat8(dl[1], dh[1]);
    MF(acc[0][1], df, hc[0]);
    if (RD) dln[1] = tr_read<0>(drd[1] + so);
    SCHED();
    MF(acc[1][1], df, hc[1]);
    if (RD) dhn[1] = tr_read<1024>(drd[1] + so);
    SCHED();
    MF(acc[2][1], df, hc[2]);
    if (RD == 1) hn[2] = ld128<2048>(hra + so);
    if (RD == 2) hn[2] = ld2x64<4>(hra2 + so);
    SCHED();
    MF(acc[3][1], df, hc[3]);
    if (RD == 1) hn[3] = ld128<3072>(hra + so);
    if (RD == 2) hn[3] = ld2x64<6>(hra2 + so);
    SCHED();
    df = cat8(dl[2], dh[2]);
    MF(acc[0][2], df, hc[0]);
    if (RD) dln[2] = tr_read<0>(drd[2] + so);
    SCHED();
    MF(acc[1][2], df, hc[1]);
    if (RD) dhn[2] = tr_read<1024>(drd[2] + so);
    SCHED();
    MF(acc[2][2], df, hc[2]);
    if (RD) dln[3] = tr_read<0>(drd[3] + so);
    SCHED();
    MF(acc[3][2], df, hc[3]);
    if (RD) dhn[3] = tr_read<1024>(drd[3] + so);
    SCHED();
    df = cat8(dl[3], dh[3]);
    MF(acc[0][3], df, hc[0]);
    if (LL) glds16c(hb + (size_t)S * (128 * 1024) + voffH, l + r * 2048);
    SCHED();
    MF(acc[1][3], df, hc[1]);
    if (LL) glds16c(hb + (size_t)S * (128 * 1024) + 1024 + voffH, l + r * 2048 + 1024);
    SCHED();
    MF(acc[2][3], df, hc[2]);
    if (LL) glds16(db + (size_t)t0 * (16 * D_MODEL * 2) + voffD, l + 8192 + r * 2048);
    SCHED();
    MF(acc[3][3], df, hc[3]);
    if (LL) glds16(db + (size_t)t1 * (16 * D_MODEL * 2) + voffD, l + 8192 + r * 2048 + 1024);
    SCHED();
    rs = (rs == 4) ? 0 : rs + 1;
    ws = (ws == 4) ? 0 : ws + 1;
  };
#define G2_STEP_A(i, a, b, c) step2(i, IC<a>{}, IC<b>{}, IC<c>{}, ha, hb2, dla, dha, dlb, dhb)
#define G2_STEP_B(i, a, b, c) step2(i, IC<a>{}, IC<b>{}, IC<c>{}, hb2, ha, dlb, dhb, dla, dha)
  unsigned long long pv;
  auto poll_issue = [&](gu64* f) __attribute__((always_inline)) {
    asm volatile("global_load_dwordx2 %0, %1, off sc1" : "=v"(pv) : "v"(f) : "memory");
  };
  auto poll_check = [&](gu64* f) __attribute__((always_inline)) {
    asm volatile("" : "+v"(pv));
    if (!__all(pv == MAGIC)) wait_flags(f);
    if (lane < 16) __hip_atomic_store(f, 0ull, __ATOMIC_RELAXED, __HIP_MEMORY_SCOPE_AGENT);
    __builtin_amdgcn_s_waitcnt(0xC07F);
  };
  G2_STEP_A(0, 1, 6, 1);
  __builtin_amdgcn_raw_buffer_store_b64(hs[0], rs_h, hso, 0, 16);
  __builtin_amdgcn_raw_buffer_store_b64(hs[1], rs_h, hso + 1024, 0, 16);
  G2_STEP_B(1, 1, 10, 1);
  __builtin_amdgcn_raw_buffer_store_b64(hs[2], rs_h, hso + 2048, 0, 16);
  __builtin_amdgcn_raw_buffer_store_b64(hs[3], rs_h, hso + 3072, 0, 16);
  G2_STEP_A(2, 1, 14, 1);
  __builtin_amdgcn_raw_buffer_store_b64(hmx[0], rs_h, hm, 0, 16);
  __builtin_amdgcn_raw_buffer_store_b64(hmx[1], rs_h, hm + 2048, 0, 16);
  STAMP_NOW();
  G2_STEP_B(3, 1, 18, 1);
  G2_STEP_A(4, 1, 18, 1);
  G2_STEP_B(5, 1, 16, 1);
  G2_STEP_A(6, 1, 14, 1);
  G2_STEP_B(7, 1, 12, 1);
  if (wid == 0 && lane < 16) __hip_atomic_store(fout + 16, MAGIC, __ATOMIC_RELAXED, __HIP_MEMORY_SCOPE_AGENT);
  G2_STEP_A(8, 1, 12, 1);
  G2_STEP_B(9, 1, 12, 1);
  if (wid == 0) poll_issue(fin + 16);
  G2_STEP_A(10, 1, 12, 1);
  STAMP_NOW();
  if (wid == 0) {
    WAIT_V(4);
    poll_check(fin + 16);
  }
  asm volatile("" ::: "memory");
  STAMP_NOW();
  G2_STEP_B(11, 1, 12, 1);
  G2_STEP_A(12, 1, 12, 1);
  G2_STEP_B(13, 1, 12, 1);
  G2_STEP_A(14, 1, 12, 1);
  G2_STEP_B(15, 1, 12, 2);
  for (int i = 16; i < 22; i += 2) {
    G2_STEP_A(i, 1, 12, 2);
    G2_STEP_B(i + 1, 1, 12, 2);
  }
  G2_STEP_A(22, 1, 12, 2);
  G2_STEP_B(23, 0, 12, 2);
  G2_STEP_A(24, 0, 8, 2);
  G2_STEP_B(25, 0, 4, 2);
  G2_STEP_A(26, 0, 0, 2);
  G2_STEP_B(27, 0, 0, 0);
  BAR();
  STAMP_NOW();

  f32x4 k0[4], k1[4];
  {
    lds_u8* dst = lds + ((1 - H) * 4 + r) * 8192 + lane * 16;
    if (H) {
#pragma unroll
      for (int u = 0; u < 4; ++u) {
        *(LAS f32x4*)(dst + u * 1024) = acc[0][u];
        *(LAS f32x4*)(dst + (4 + u) * 1024) = acc[1][u];
        k0[u] = acc[2][u]; k1[u] = acc[3][u];
      }
    } else {
#pragma unroll
      for (int u = 0; u < 4; ++u) {
        *(LAS f32x4*)(dst + u * 1024) = acc[2][u];
        *(LAS f32x4*)(dst + (4 + u) * 1024) = acc[3][u];
        k0[u] = acc[0][u]; k1[u] = acc[1][u];
      }
    }
  }
  __syncthreads();
  f32x4 keep[4], send[4];
  {
    lds_u8* src = lds + wid * 8192 + lane * 16;
#pragma unroll
    for (int u = 0; u < 4; ++u) {
      k0[u] += *(LAS f32x4*)(src + u * 1024);
      k1[u] += *(LAS f32x4*)(src + (4 + u) * 1024);
    }
    if (kh) {
#pragma unroll
      for (int u = 0; u < 4; ++u) { keep[u] = k1[u]; send[u] = k0[u]; }
    } else {
#pragma unroll
      for (int u = 0; u < 4; ++u) { keep[u] = k0[u]; send[u] = k1[u]; }
    }
  }
  STAMP_NOW();
  {
    const int pb = bid ^ 2;
    auto rs_x = __builtin_amdgcn_make_buffer_rsrc(hbuf + WS_XCH, 0, 256 * 32768, 0x00020000);
    const unsigned so = (unsigned)(pb * 32768 + wid * 4096 + lane * 16);
#pragma unroll
    for (int u = 0; u < 4; ++u) __builtin_amdgcn_raw_buffer_store_b128(__builtin_bit_cast(u32x4, send[u]), rs_x, so + u * 1024, 0, 16);
    WAIT_V(0);
    BAR();
    STAMP_NOW();
    if (wid == 0) {
      gu64* fl_out = (gu64*)(hbuf + WS_FLAG + (size_t)pb * 512 + 384);
      gu64* fl_in = (gu64*)(hbuf + WS_FLAG + (size_t)bid * 512 + 384);
      if (lane == 0) __hip_atomic_store(fl_out, MAGIC, __ATOMIC_RELAXED, __HIP_MEMORY_SCOPE_AGENT);
      const unsigned long long t0 = __builtin_amdgcn_s_memrealtime();
      for (;;) {
        const bool ok = __hip_atomic_load(fl_in, __ATOMIC_RELAXED, __HIP_MEMORY_SCOPE_AGENT) == MAGIC;
        if (__all(ok)) break;
        if (__builtin_amdgcn_s_memrealtime() - t0 > 5000000ull) break;
        __builtin_amdgcn_s_sleep(1);
      }
      if (lane == 0) __hip_atomic_store(fl_in, 0ull, __ATOMIC_RELAXED, __HIP_MEMORY_SCOPE_AGENT);
    }
    asm volatile("" ::: "memory");
    BAR();
    asm volatile("" ::: "memory");
    const unsigned si = (unsigned)(bid * 32768 + wid * 4096 + lane * 16);
#pragma unroll
    for (int u = 0; u < 4; ++u) keep[u] += __builtin_bit_cast(f32x4, __builtin_amdgcn_raw_buffer_load_b128(rs_x, si + u * 1024, 0, 16));
  }
  unsigned char* op = out + ((size_t)(mt2 * 128 + wm2 * 64 + (2 * H + kh) * 16 + tok) * D_MODEL + nt2 * 128 + wn2 * 64) * 2;
#pragma unroll
  for (int u = 0; u < 4; u += 2) {
    unsigned a0 = cvt_pk_bf16(keep[u][0], keep[u][1]), a1 = cvt_pk_bf16(keep[u][2], keep[u][3]);
    unsigned b0 = cvt_pk_bf16(keep[u + 1][0], keep[u + 1][1]), b1 = cvt_pk_bf16(keep[u + 1][2], keep[u + 1][3]);
    auto s0 = __builtin_amdgcn_permlane16_swap(a0, b0, false, false);
    auto s1 = __builtin_amdgcn_permlane16_swap(a1, b1, false, false);
    u32x4 v = {s0[0], s1[0], s0[1], s1[1]};
    const int col = (g & 1) ? (16 * (u + 1) + 4 * (g - 1)) : (16 * u + 4 * g);
    *(u32x4*)(op + col * 2) = v;
  }
  STAMP_NOW();
  STAMP_NOW();
  STAMP_OUT(hbuf + (64u << 20));
}

extern "C" void kernel_launch(void* const* d_in, const int* in_sizes, int n_in, void* d_out, int out_size, void* d_ws,
                              size_t ws_size, hipStream_t stream) {
  const unsigned char* x = (const unsigned char*)d_in[0];
  const unsigned char* wg = (const unsigned char*)d_in[1];
  const unsigned char* wu = (const unsigned char*)d_in[2];
  const unsigned char* wd = (const unsigned char*)d_in[3];
  unsigned char* hbuf = (unsigned char*)d_ws;
  (void)hipFuncSetAttribute((const void*)k_fused, hipFuncAttributeMaxDynamicSharedMemorySize, G1_LDS);
  k_fused<<<256, 512, G1_LDS, stream>>>(x, wg, wu, hbuf, wd, (unsigned char*)d_out);
}
```

```cpp
#include <hip/hip_runtime.h>
#include <hip/hip_bf16.h>
#include <stdint.h>
#define PF3 1

typedef short bf16x8 __attribute__((ext_vector_type(8)));
typedef short bf16x4 __attribute__((ext_vector_type(4)));
typedef float f32x4 __attribute__((ext_vector_type(4)));
typedef float f32x2 __attribute__((ext_vector_type(2)));
typedef unsigned u32x2 __attribute__((ext_vector_type(2)));
typedef unsigned u32x4 __attribute__((ext_vector_type(4)));
#define LAS __attribute__((address_space(3)))
typedef LAS unsigned char lds_u8;

constexpr int M_TOK = 2048, D_MODEL = 1024, F_FF = 3584;
constexpr size_t WS_XCH = 16u << 20;
constexpr size_t WS_FLAG = 32u << 20;

#define WAIT_V(n) asm volatile("s_waitcnt vmcnt(%0)" ::"n"(n) : "memory")
#define WAIT_L0() asm volatile("s_waitcnt lgkmcnt(0)" ::: "memory")
#define BAR() __builtin_amdgcn_s_barrier()
#define SCHED() __builtin_amdgcn_sched_barrier(0)
#define FINL __device__ __forceinline__

#define STAMP_DECL
#define STAMP_NOW()
#define STAMP_OUT(base)
template <int N> struct IC { static constexpr int value = N; };

FINL void glds16c(const void* g, lds_u8* l) {
  __builtin_amdgcn_global_load_lds((const unsigned*)g, (LAS unsigned*)l, 16, 0, 16);
}
FINL void glds16(const void* g, lds_u8* l) {
  __builtin_amdgcn_global_load_lds((const unsigned*)g, (LAS unsigned*)l, 16, 0, 0);
}
template <int OFF> FINL bf16x4 tr_read(unsigned addr) {
  bf16x4 r;
  asm volatile("ds_read_b64_tr_b16 %0, %1 offset:%2" : "=v"(r) : "v"(addr), "n"(OFF) : "memory");
  return r;
}
template <int OFF> FINL bf16x8 ld128(unsigned addr) {
  bf16x8 r;
  asm volatile("ds_read_b128 %0, %1 offset:%2" : "=v"(r) : "v"(addr), "n"(OFF) : "memory");
  return r;
}
template <int O0> FINL bf16x8 ld2x64(unsigned addr) {
  bf16x8 r;
  asm volatile("ds_read2st64_b64 %0, %1 offset0:%2 offset1:%3" : "=v"(r) : "v"(addr), "n"(O0), "n"(O0 + 1) : "memory");
  return r;
}
#define WAIT_L(n) asm volatile("s_waitcnt lgkmcnt(%0)" ::"n"(n) : "memory")
FINL bf16x8 cat8(bf16x4 a, bf16x4 b) { return __builtin_shufflevector(a, b, 0, 1, 2, 3, 4, 5, 6, 7); }
FINL unsigned cvt_pk_bf16(float lo, float hi) {
  unsigned r;
  asm("v_cvt_pk_bf16_f32 %0, %1, %2" : "=v"(r) : "v"(lo), "v"(hi));
  return r;
}
FINL f32x2 geglu2(f32x2 g, f32x2 u) {
  const float c0 = -2.0f * 0.7978845608028654f * 1.4426950408889634f;
  const float c1 = c0 * 0.044715f;
  f32x2 t = g * g;
  f32x2 p = t * c1 + c0;
  f32x2 a = g * p;
  f32x2 e;
  e.x = __builtin_amdgcn_exp2f(a.x);
  e.y = __builtin_amdgcn_exp2f(a.y);
  f32x2 d = e + 1.0f;
  f32x2 r;
  r.x = __builtin_amdgcn_rcpf(d.x);
  r.y = __builtin_amdgcn_rcpf(d.y);
  return (g * r) * u;
}
FINL u32x2 geglu4(f32x4 g, f32x4 u) {
  f32x2 a = geglu2((f32x2){g[0], g[1]}, (f32x2){u[0], u[1]});
  f32x2 b = geglu2((f32x2){g[2], g[3]}, (f32x2){u[2], u[3]});
  u32x2 o;
  o.x = cvt_pk_bf16(a.x, a.y);
  o.y = cvt_pk_bf16(b.x, b.y);
  return o;
}

constexpr int G1_WGRP = 2048;
constexpr int G1_WSLOT = 8 * G1_WGRP;
constexpr int G1_NW = 4;
constexpr int G1_XSLOT = 32768;
constexpr int G1_NX = 3;
constexpr int G1_XOFF = G1_NW * G1_WSLOT;
constexpr int G1_LDS = G1_XOFF + G1_NX * G1_XSLOT;

constexpr int G2_STAGE = 16384;
constexpr unsigned long long MAGIC = 0x5E1F0A3C97B2D461ull;
typedef __attribute__((address_space(1))) unsigned long long gu64;

__global__ __launch_bounds__(512, 2) void k_fused(const unsigned char* __restrict__ x, const unsigned char* __restrict__ wg,
                                                  const unsigned char* __restrict__ wu, unsigned char* hbuf,
                                                  const unsigned char* __restrict__ wd, unsigned char* __restrict__ out) {
  extern __shared__ __attribute__((aligned(16))) unsigned char smem_raw[];
  lds_u8* lds = (lds_u8*)smem_raw;
  const int tid = threadIdx.x;
  const int wid = __builtin_amdgcn_readfirstlane(tid >> 6);
  const int lane = tid & 63;
  const int half = wid >> 2, wm = wid & 3, wn = wid >> 2;
  STAMP_DECL
  STAMP_NOW();
  const int bid = blockIdx.x;
  const int xcd = bid & 7, idx = bid >> 3;
  const int mt = (xcd >> 2) * 4 + (idx & 3);
  const int nt = (xcd & 3) * 8 + (idx >> 2);
  const int g = lane >> 4, q = (lane >> 2) & 3, p = lane & 3, tok = lane & 15;

  int li = lane - ((wid >> 1) & 1) * 8;
  li = (li < 0 || li >= 56) ? 0 : li;
  const unsigned voffW = (unsigned)((4 * wid + li / 14) * (F_FF * 2) + (li % 14) * 16);
  unsigned voffX[2];
#pragma unroll
  for (int i = 0; i < 2; ++i)
    voffX[i] = (unsigned)((8 * (2 * wid + i) + (lane >> 3)) * (D_MODEL * 2) + (((lane & 7) ^ ((i << 2) | (lane >> 4))) * 16));
  const unsigned char* wgb = wg + (size_t)nt * 224;
  const unsigned char* wub = wu + (size_t)nt * 224;
  const unsigned char* xb = x + (size_t)mt * 256 * (D_MODEL * 2);
  const int wdst = wid * G1_WGRP;
  const int xdst = G1_XOFF + wid * 2048;

  const int rot2 = 0;
  auto stageW = [&](int j, int slot) __attribute__((always_inline)) {
    const size_t go = (size_t)((j + 2 * rot2) & 31) * (32 * F_FF * 2);
    glds16(wgb + go + voffW, lds + slot * G1_WSLOT + wdst);
    glds16(wub + go + voffW, lds + slot * G1_WSLOT + wdst + 1024);
  };
  auto stageX = [&](int t, int hb, int slot) __attribute__((always_inline)) {
    const size_t go = (size_t)hb * (128 * D_MODEL * 2) + (size_t)((t + rot2) & 15) * 128;
    glds16(xb + go + voffX[0], lds + xdst + slot * G1_XSLOT + hb * 16384);
    glds16(xb + go + voffX[1], lds + xdst + slot * G1_XSLOT + hb * 16384 + 1024);
  };

  const unsigned lbase = (unsigned)(size_t)lds;
  const unsigned wrd = lbase + 2 * g * G1_WGRP + (g & 1) * 128 + q * 224 + p * 8 + wn * 128;
  const unsigned wrdm = lbase + 2 * g * G1_WGRP + (g & 1) * 128 + q * 224 + 96 + wn * 16 + (p & 1) * 8 + (p >> 1) * 1024;
  int xrd[2];
#pragma unroll
  for (int s = 0; s < 2; ++s) xrd[s] = G1_XOFF + (wm * 64 + tok) * 128 + ((((s << 2) | g) ^ ((tok >> 1) & 7)) * 16);

  f32x4 accg[4][3], accu[4][3], accm[4];
#pragma unroll
  for (int t = 0; t < 4; ++t) {
#pragma unroll
    for (int u = 0; u < 3; ++u) { accg[t][u] = (f32x4){0.f, 0.f, 0.f, 0.f}; accu[t][u] = (f32x4){0.f, 0.f, 0.f, 0.f}; }
    accm[t] = (f32x4){0.f, 0.f, 0.f, 0.f};
  }

  const int H = (wid >> 2) & 1, r = wid & 3;
  const int kh = (xcd >> 1) & 1;
  const int mt2 = (xcd >> 2) * 8 + (idx & 7);
  const int nt2 = (xcd & 1) * 4 + (idx >> 3);
  const unsigned char* hb = hbuf + ((size_t)kh * 56 * 128 + mt2 * 8 + 2 * r) * 1024;
  const unsigned char* db = wd + (size_t)kh * 56 * 32 * (D_MODEL * 2) + (size_t)nt2 * 256;
  stageX(0, 0, 0); stageX(0, 1, 0); stageW(0, 0);
  stageW(1, 1); stageX(1, 0, 1);
  stageW(2, 2); stageX(1, 1, 1);
  stageW(3, 3); stageX(2, 0, 2);
  WAIT_V(12);
  BAR();
  STAMP_NOW();

  const unsigned xra[2] = {lbase + (unsigned)xrd[0], lbase + (unsigned)xrd[1]};
  bf16x4 wl[7], wh[7];
  bf16x8 xa[4], xb2[4];
  {
    wl[0] = tr_read<0>(wrd); wh[0] = tr_read<G1_WGRP>(wrd);
    wl[1] = tr_read<32>(wrd); wh[1] = tr_read<32 + G1_WGRP>(wrd);
    wl[2] = tr_read<64>(wrd); wh[2] = tr_read<64 + G1_WGRP>(wrd);
    wl[3] = tr_read<1024>(wrd); wh[3] = tr_read<1024 + G1_WGRP>(wrd);
    wl[4] = tr_read<1024 + 32>(wrd); wh[4] = tr_read<1024 + 32 + G1_WGRP>(wrd);
    wl[5] = tr_read<1024 + 64>(wrd); wh[5] = tr_read<1024 + 64 + G1_WGRP>(wrd);
    wl[6] = tr_read<0>(wrdm); wh[6] = tr_read<G1_WGRP>(wrdm);
    xa[0] = ld128<0>(xra[0]); xa[1] = ld128<2048>(xra[0]); xa[2] = ld128<4096>(xra[0]); xa[3] = ld128<6144>(xra[0]);
    WAIT_L(0);
  }
  int xs_rd = 0, xs_wr = 2;
  auto step = [&](int k, auto S_, auto IW_, auto IX_, auto VM_, auto RD_, bf16x8(&xc)[4], bf16x8(&xn)[4], auto IP_) __attribute__((always_inline)) {
    constexpr int S = decltype(S_)::value;
    constexpr int IP = decltype(IP_)::value;
    constexpr int IW = decltype(IW_)::value, IX = decltype(IX_)::value, VM = decltype(VM_)::value, RD = decltype(RD_)::value;
    if (S) xs_rd = (xs_rd == 2) ? 0 : xs_rd + 1;
    const unsigned wb = ((k + 1) & 3) * G1_WSLOT + wrd;
    const unsigned wbm = ((k + 1) & 3) * G1_WSLOT + wrdm;
    const unsigned xbp = xs_rd * G1_XSLOT + xra[S ^ 1];
#define MF(ACC, WF, XF) ACC = __builtin_amdgcn_mfma_f32_16x16x32_bf16(WF, XF, ACC, 0, 0, 0); SCHED();
    constexpr int LW = IW, LX = IX;
    const size_t goW = (size_t)((k + 4 + 2 * rot2) & 31) * (32 * F_FF * 2);
    const size_t goX = (size_t)(S ^ 1) * (128 * D_MODEL * 2) + (size_t)((((k + 1) >> 1) + 2 + rot2) & 15) * 128;
    lds_u8* ldW = lds + (k & 3) * G1_WSLOT + wdst;
    lds_u8* ldX = lds + xdst + xs_wr * G1_XSLOT + (S ^ 1) * 16384;
    bf16x8 wf;
    WAIT_L(10);
    SCHED();
    wf = cat8(wl[0], wh[0]);
    MF(accg[0][0], wf, xc[0]);
    MF(accg[1][0], wf, xc[1]);
    MF(accg[2][0], wf, xc[2]);
    MF(accg[3][0], wf, xc[3]);
    WAIT_L(0);
    if (RD) {
      if (VM >= 0) WAIT_V(VM);
      BAR();
    }
    SCHED();
    wf = cat8(wl[3], wh[3]);
    MF(accu[0][0], wf, xc[0]);
    if (RD) wl[0] = tr_read<0>(wb);
    SCHED();
    MF(accu[1][0], wf, xc[1]);
    if (RD) wh[0] = tr_read<0 + G1_WGRP>(wb);
    SCHED();
    MF(accu[2][0], wf, xc[2]);
    if (RD) xn[0] = ld128<0>(xbp);
    SCHED();
    MF(accu[3][0], wf, xc[3]);
    if (RD) xn[1] = ld128<2048>(xbp);
    SCHED();
    wf = cat8(wl[1], wh[1]);
    MF(accg[0][1], wf, xc[0]);
    if (RD) wl[3] = tr_read<1024>(wb);
    SCHED();
    MF(accg[1][1], wf, xc[1]);
    if (RD) wh[3] = tr_read<1024 + G1_WGRP>(wb);
    SCHED();
    MF(accg[2][1], wf, xc[2]);
    if (RD) xn[2] = ld128<4096>(xbp);
    SCHED();
    MF(accg[3][1], wf, xc[3]);
    if (RD) xn[3] = ld128<6144>(xbp);
    SCHED();
    wf = cat8(wl[4], wh[4]);
    MF(accu[0][1], wf, xc[0]);
    if (RD) wl[1] = tr_read<32>(wb);
    SCHED();
    MF(accu[1][1], wf, xc[1]);
    if (RD) wh[1] = tr_read<32 + G1_WGRP>(wb);
    SCHED();
    MF(accu[2][1], wf, xc[2]);
    if (LW) glds16(wgb + goW + voffW, ldW);
    SCHED();
    MF(accu[3][1], wf, xc[3]);
    wf = cat8(wl[2], wh[2]);
    MF(accg[0][2], wf, xc[0]);
    if (RD) wl[4] = tr_read<1024 + 32>(wb);
    SCHED();
    MF(accg[1][2], wf, xc[1]);
    if (RD) wh[4] = tr_read<1024 + 32 + G1_WGRP>(wb);
    SCHED();
    MF(accg[2][2], wf, xc[2]);
    if (LW) glds16(wub + goW + voffW, ldW + 1024);
    SCHED();
    MF(accg[3][2], wf, xc[3]);
    wf = cat8(wl[5], wh[5]);
    MF(accu[0][2], wf, xc[0]);
    if (RD) wl[2] = tr_read<64>(wb);
    SCHED();
    MF(accu[1][2], wf, xc[1]);
    if (RD) wh[2] = tr_read<64 + G1_WGRP>(wb);
    SCHED();
    MF(accu[2][2], wf, xc[2]);
    if (LX) glds16(xb + goX + voffX[0], ldX);
    SCHED();
    MF(accu[3][2], wf, xc[3]);
    wf = cat8(wl[6], wh[6]);
    MF(accm[0], wf, xc[0]);
    if (RD) wl[5] = tr_read<1024 + 64>(wb);
    SCHED();
    MF(accm[1], wf, xc[1]);
    if (RD) wh[5] = tr_read<1024 + 64 + G1_WGRP>(wb);
    SCHED();
    MF(accm[2], wf, xc[2]);
    if (LX) glds16(xb + goX + voffX[1], ldX + 1024);
    SCHED();
    MF(accm[3], wf, xc[3]);
    if (RD) wl[6] = tr_read<0>(wbm);
    if (RD) wh[6] = tr_read<0 + G1_WGRP>(wbm);
    SCHED();
    if (!S) xs_wr = (xs_wr == 2) ? 0 : xs_wr + 1;
  };
  for (int k = 0; k < 26; k += 2) {
    step(k, IC<0>{}, IC<1>{}, IC<1>{}, IC<8>{}, IC<1>{}, xa, xb2, IC<0>{});
    step(k + 1, IC<1>{}, IC<1>{}, IC<1>{}, IC<8>{}, IC<1>{}, xb2, xa, IC<0>{});
  }
  step(26, IC<0>{}, IC<1>{}, IC<1>{}, IC<8>{}, IC<1>{}, xa, xb2, IC<0>{});
  step(27, IC<1>{}, IC<1>{}, IC<0>{}, IC<8>{}, IC<1>{}, xb2, xa, IC<0>{});
  {
    bf16x8 wf;
    WAIT_L(0);
    WAIT_V(6);
    BAR();
    SCHED();
    wf = cat8(wl[0], wh[0]);
    MF(accg[0][0], wf, xa[0]);
    xb2[0] = ld128<0>((xra[1] + 2 * G1_XSLOT));
    SCHED();
    MF(accg[1][0], wf, xa[1]);
    xb2[1] = ld128<2048>((xra[1] + 2 * G1_XSLOT));
    SCHED();
    MF(accg[2][0], wf, xa[2]);
    xb2[2] = ld128<4096>((xra[1] + 2 * G1_XSLOT));
    SCHED();
    MF(accg[3][0], wf, xa[3]);
    xb2[3] = ld128<6144>((xra[1] + 2 * G1_XSLOT));
    SCHED();
    wf = cat8(wl[3], wh[3]);
    MF(accu[0][0], wf, xa[0]);
    wl[0] = tr_read<0>((wrd + 1 * G1_WSLOT));
    SCHED();
    MF(accu[1][0], wf, xa[1]);
    wh[0] = tr_read<0 + G1_WGRP>((wrd + 1 * G1_WSLOT));
    SCHED();
    MF(accu[2][0], wf, xa[2]);
    MF(accu[3][0], wf, xa[3]);
    wf = cat8(wl[1], wh[1]);
    MF(accg[0][1], wf, xa[0]);
    wl[3] = tr_read<1024>((wrd + 1 * G1_WSLOT));
    SCHED();
    MF(accg[1][1], wf, xa[1]);
    wh[3] = tr_read<1024 + G1_WGRP>((wrd + 1 * G1_WSLOT));
    SCHED();
    MF(accg[2][1], wf, xa[2]);
    MF(accg[3][1], wf, xa[3]);
    wf = cat8(wl[4], wh[4]);
    MF(accu[0][1], wf, xa[0]);
    wl[1] = tr_read<32>((wrd + 1 * G1_WSLOT));
    SCHED();
    MF(accu[1][1], wf, xa[1]);
    wh[1] = tr_read<32 + G1_WGRP>((wrd + 1 * G1_WSLOT));
    SCHED();
    MF(accu[2][1], wf, xa[2]);
    MF(accu[3][1], wf, xa[3]);
    wl[4] = tr_read<1024 + 32>((wrd + 1 * G1_WSLOT));
    wh[4] = tr_read<1024 + 32 + G1_WGRP>((wrd + 1 * G1_WSLOT));
    SCHED();
  }
  {
    bf16x8 wf;
    WAIT_L(0);
    WAIT_V(2);
    BAR();
    SCHED();
    wf = cat8(wl[0], wh[0]);
    MF(accg[0][0], wf, xb2[0]);
    xa[0] = ld128<0>((xra[0] + 0 * G1_XSLOT));
    SCHED();
    MF(accg[1][0], wf, xb2[1]);
    xa[1] = ld128<2048>((xra[0] + 0 * G1_XSLOT));
    SCHED();
    MF(accg[2][0], wf, xb2[2]);
    xa[2] = ld128<4096>((xra[0] + 0 * G1_XSLOT));
    SCHED();
    MF(accg[3][0], wf, xb2[3]);
    xa[3] = ld128<6144>((xra[0] + 0 * G1_XSLOT));
    SCHED();
    wf = cat8(wl[3], wh[3]);
    MF(accu[0][0], wf, xb2[0]);
    wl[0] = tr_read<0>((wrd + 2 * G1_WSLOT));
    SCHED();
    MF(accu[1][0], wf, xb2[1]);
    wh[0] = tr_read<0 + G1_WGRP>((wrd + 2 * G1_WSLOT));
    SCHED();
    MF(accu[2][0], wf, xb2[2]);
    MF(accu[3][0], wf, xb2[3]);
    wf = cat8(wl[1], wh[1]);
    MF(accg[0][1], wf, xb2[0]);
    wl[3] = tr_read<1024>((wrd + 2 * G1_WSLOT));
    SCHED();
    MF(accg[1][1], wf, xb2[1]);
    wh[3] = tr_read<1024 + G1_WGRP>((wrd + 2 * G1_WSLOT));
    SCHED();
    MF(accg[2][1], wf, xb2[2]);
    MF(accg[3][1], wf, xb2[3]);
    wf = cat8(wl[4], wh[4]);
    MF(accu[0][1], wf, xb2[0]);
    wl[1] = tr_read<32>((wrd + 2 * G1_WSLOT));
    SCHED();
    MF(accu[1][1], wf, xb2[1]);
    wh[1] = tr_read<32 + G1_WGRP>((wrd + 2 * G1_WSLOT));
    SCHED();
    MF(accu[2][1], wf, xb2[2]);
    MF(accu[3][1], wf, xb2[3]);
    wl[4] = tr_read<1024 + 32>((wrd + 2 * G1_WSLOT));
    wh[4] = tr_read<1024 + 32 + G1_WGRP>((wrd + 2 * G1_WSLOT));
    SCHED();
  }
  {
    bf16x8 wf;
    WAIT_L(0);
    WAIT_V(0);
    BAR();
    SCHED();
    wf = cat8(wl[0], wh[0]);
    MF(accg[0][0], wf, xa[0]);
    xb2[0] = ld128<0>((xra[1] + 0 * G1_XSLOT));
    SCHED();
    MF(accg[1][0], wf, xa[1]);
    xb2[1] = ld128<2048>((xra[1] + 0 * G1_XSLOT));
    SCHED();
    MF(accg[2][0], wf, xa[2]);
    xb2[2] = ld128<4096>((xra[1] + 0 * G1_XSLOT));
    SCHED();
    MF(accg[3][0], wf, xa[3]);
    xb2[3] = ld128<6144>((xra[1] + 0 * G1_XSLOT));
    SCHED();
    wf = cat8(wl[3], wh[3]);
    MF(accu[0][0], wf, xa[0]);
    wl[0] = tr_read<0>((wrd + 3 * G1_WSLOT));
    SCHED();
    MF(accu[1][0], wf, xa[1]);
    wh[0] = tr_read<0 + G1_WGRP>((wrd + 3 * G1_WSLOT));
    SCHED();
    MF(accu[2][0], wf, xa[2]);
    MF(accu[3][0], wf, xa[3]);
    wf = cat8(wl[1], wh[1]);
    MF(accg[0][1], wf, xa[0]);
    wl[3] = tr_read<1024>((wrd + 3 * G1_WSLOT));
    SCHED();
    MF(accg[1][1], wf, xa[1]);
    wh[3] = tr_read<1024 + G1_WGRP>((wrd + 3 * G1_WSLOT));
    SCHED();
    MF(accg[2][1], wf, xa[2]);
    MF(accg[3][1], wf, xa[3]);
    wf = cat8(wl[4], wh[4]);
    MF(accu[0][1], wf, xa[0]);
    wl[1] = tr_read<32>((wrd + 3 * G1_WSLOT));
    SCHED();
    MF(accu[1][1], wf, xa[1]);
    wh[1] = tr_read<32 + G1_WGRP>((wrd + 3 * G1_WSLOT));
    SCHED();
    MF(accu[2][1], wf, xa[2]);
    MF(accu[3][1], wf, xa[3]);
    wl[4] = tr_read<1024 + 32>((wrd + 3 * G1_WSLOT));
    wh[4] = tr_read<1024 + 32 + G1_WGRP>((wrd + 3 * G1_WSLOT));
    SCHED();
  }
  {
    bf16x8 wf;
    WAIT_L(0);
    SCHED();
    wf = cat8(wl[0], wh[0]);
    MF(accg[0][0], wf, xb2[0]);
    xa[0] = ld128<0>((xra[0] + 2 * G1_XSLOT));
    SCHED();
    MF(accg[1][0], wf, xb2[1]);
    xa[1] = ld128<2048>((xra[0] + 2 * G1_XSLOT));
    SCHED();
    MF(accg[2][0], wf, xb2[2]);
    xa[2] = ld128<4096>((xra[0] + 2 * G1_XSLOT));
    SCHED();
    MF(accg[3][0], wf, xb2[3]);
    xa[3] = ld128<6144>((xra[0] + 2 * G1_XSLOT));
    SCHED();
    wf = cat8(wl[3], wh[3]);
    MF(accu[0][0], wf, xb2[0]);
    MF(accu[1][0], wf, xb2[1]);
    MF(accu[2][0], wf, xb2[2]);
    MF(accu[3][0], wf, xb2[3]);
    wf = cat8(wl[1], wh[1]);
    MF(accg[0][1], wf, xb2[0]);
    MF(accg[1][1], wf, xb2[1]);
    MF(accg[2][1], wf, xb2[2]);
    MF(accg[3][1], wf, xb2[3]);
    wf = cat8(wl[4], wh[4]);
    MF(accu[0][1], wf, xb2[0]);
    MF(accu[1][1], wf, xb2[1]);
    MF(accu[2][1], wf, xb2[2]);
    MF(accu[3][1], wf, xb2[3]);
    SCHED();
  }
  STAMP_NOW();
  {
  int tz = threadIdx.x;
  asm volatile("" : "+v"(tz));
  const int lane = tz & 63, g = lane >> 4, q = (lane >> 2) & 3, p = lane & 3, tok = lane & 15;
  auto rs_h = __builtin_amdgcn_make_buffer_rsrc(hbuf, 0, 112 * 128 * 1024, 0x00020000);
  const int T0 = mt * 16 + wm * 4;
  const int sbase = (nt >> 4) * 56, jj = nt & 15;
  {
    const unsigned ho = (unsigned)(((sbase + 2 * jj + wn) * 128 + T0) * 1024 + lane * 16);
#pragma unroll
    for (int t = 0; t < 4; ++t) {
      const u32x2 a = geglu4(accg[t][0], accu[t][0]), b = geglu4(accg[t][1], accu[t][1]);
      __builtin_amdgcn_raw_buffer_store_b128((u32x4){a.x, a.y, b.x, b.y}, rs_h, ho + t * 1024, 0, 16);
    }
  }
  SCHED();
  STAMP_NOW();
  {
    bf16x8 wf;
    WAIT_L(0);
    SCHED();
    wf = cat8(wl[2], wh[2]);
    MF(accg[0][2], wf, xa[0]);
    xb2[0] = ld128<0>((xra[1] + 2 * G1_XSLOT));
    SCHED();
    MF(accg[1][2], wf, xa[1]);
    xb2[1] = ld128<2048>((xra[1] + 2 * G1_XSLOT));
    SCHED();
    MF(accg[2][2], wf, xa[2]);
    xb2[2] = ld128<4096>((xra[1] + 2 * G1_XSLOT));
    SCHED();
    MF(accg[3][2], wf, xa[3]);
    xb2[3] = ld128<6144>((xra[1] + 2 * G1_XSLOT));
    SCHED();
    wf = cat8(wl[5], wh[5]);
    MF(accu[0][2], wf, xa[0]);
    wl[2] = tr_read<64>((wrd + 1 * G1_WSLOT));
    SCHED();
    MF(accu[1][2], wf, xa[1]);
    wh[2] = tr_read<64 + G1_WGRP>((wrd + 1 * G1_WSLOT));
    SCHED();
    MF(accu[2][2], wf, xa[2]);
    MF(accu[3][2], wf, xa[3]);
    wf = cat8(wl[6], wh[6]);
    MF(accm[0], wf, xa[0]);
    wl[5] = tr_read<1024 + 64>((wrd + 1 * G1_WSLOT));
    SCHED();
    MF(accm[1], wf, xa[1]);
    wh[5] = tr_read<1024 + 64 + G1_WGRP>((wrd + 1 * G1_WSLOT));
    SCHED();
    MF(accm[2], wf, xa[2]);
    MF(accm[3], wf, xa[3]);
    wl[6] = tr_read<0>((wrdm + 1 * G1_WSLOT));
    wh[6] = tr_read<0 + G1_WGRP>((wrdm + 1 * G1_WSLOT));
    SCHED();
  }
  {
    bf16x8 wf;
    WAIT_L(0);
    SCHED();
    wf = cat8(wl[2], wh[2]);
    MF(accg[0][2], wf, xb2[0]);
    xa[0] = ld128<0>((xra[0] + 0 * G1_XSLOT));
    SCHED();
    MF(accg[1][2], wf, xb2[1]);
    xa[1] = ld128<2048>((xra[0] + 0 * G1_XSLOT));
    SCHED();
    MF(accg[2][2], wf, xb2[2]);
    xa[2] = ld128<4096>((xra[0] + 0 * G1_XSLOT));
    SCHED();
    MF(accg[3][2], wf, xb2[3]);
    xa[3] = ld128<6144>((xra[0] + 0 * G1_XSLOT));
    SCHED();
    wf = cat8(wl[5], wh[5]);
    MF(accu[0][2], wf, xb2[0]);
    wl[2] = tr_read<64>((wrd + 2 * G1_WSLOT));
    SCHED();
    MF(accu[1][2], wf, xb2[1]);
    wh[2] = tr_read<64 + G1_WGRP>((wrd + 2 * G1_WSLOT));
    SCHED();
    MF(accu[2][2], wf, xb2[2]);
    MF(accu[3][2], wf, xb2[3]);
    wf = cat8(wl[6], wh[6]);
    MF(accm[0], wf, xb2[0]);
    wl[5] = tr_read<1024 + 64>((wrd + 2 * G1_WSLOT));
    SCHED();
    MF(accm[1], wf, xb2[1]);
    wh[5] = tr_read<1024 + 64 + G1_WGRP>((wrd + 2 * G1_WSLOT));
    SCHED();
    MF(accm[2], wf, xb2[2]);
    MF(accm[3], wf, xb2[3]);
    wl[6] = tr_read<0>((wrdm + 2 * G1_WSLOT));
    wh[6] = tr_read<0 + G1_WGRP>((wrdm + 2 * G1_WSLOT));
    SCHED();
  }
  {
    bf16x8 wf;
    WAIT_L(0);
    SCHED();
    wf = cat8(wl[2], wh[2]);
    MF(accg[0][2], wf, xa[0]);
    xb2[0] = ld128<0>((xra[1] + 0 * G1_XSLOT));
    SCHED();
    MF(accg[1][2], wf, xa[1]);
    xb2[1] = ld128<2048>((xra[1] + 0 * G1_XSLOT));
    SCHED();
    MF(accg[2][2], wf, xa[2]);
    xb2[2] = ld128<4096>((xra[1] + 0 * G1_XSLOT));
    SCHED();
    MF(accg[3][2], wf, xa[3]);
    xb2[3] = ld128<6144>((xra[1] + 0 * G1_XSLOT));
    SCHED();
    wf = cat8(wl[5], wh[5]);
    MF(accu[0][2], wf, xa[0]);
    wl[2] = tr_read<64>((wrd + 3 * G1_WSLOT));
    SCHED();
    MF(accu[1][2], wf, xa[1]);
    wh[2] = tr_read<64 + G1_WGRP>((wrd + 3 * G1_WSLOT));
    SCHED();
    MF(accu[2][2], wf, xa[2]);
    MF(accu[3][2], wf, xa[3]);
    wf = cat8(wl[6], wh[6]);
    MF(accm[0], wf, xa[0]);
    wl[5] = tr_read<1024 + 64>((wrd + 3 * G1_WSLOT));
    SCHED();
    MF(accm[1], wf, xa[1]);
    wh[5] = tr_read<1024 + 64 + G1_WGRP>((wrd + 3 * G1_WSLOT));
    SCHED();
    MF(accm[2], wf, xa[2]);
    MF(accm[3], wf, xa[3]);
    wl[6] = tr_read<0>((wrdm + 3 * G1_WSLOT));
    wh[6] = tr_read<0 + G1_WGRP>((wrdm + 3 * G1_WSLOT));
    SCHED();
  }
  {
    bf16x8 wf;
    WAIT_L(0);
    SCHED();
    wf = cat8(wl[2], wh[2]);
    MF(accg[0][2], wf, xb2[0]);
    MF(accg[1][2], wf, xb2[1]);
    MF(accg[2][2], wf, xb2[2]);
    MF(accg[3][2], wf, xb2[3]);
    wf = cat8(wl[5], wh[5]);
    MF(accu[0][2], wf, xb2[0]);
    MF(accu[1][2], wf, xb2[1]);
    MF(accu[2][2], wf, xb2[2]);
    MF(accu[3][2], wf, xb2[3]);
    wf = cat8(wl[6], wh[6]);
    MF(accm[0], wf, xb2[0]);
    MF(accm[1], wf, xb2[1]);
    MF(accm[2], wf, xb2[2]);
    MF(accm[3], wf, xb2[3]);
    SCHED();
  }
  WAIT_V(0);
  BAR();
  STAMP_NOW();
  const unsigned voffH = (unsigned)(lane * 16);
  const unsigned voffD = (unsigned)((4 * r + g) * (D_MODEL * 2) + ((((tok >> 1) ^ (g | ((r & 1) << 2))) << 1) | (lane & 1)) * 16);
  gu64* fout;
  gu64* fin = (gu64*)(hbuf + WS_FLAG + (size_t)bid * 512 + (lane & 15) * 8);
  {
    const int cm = 2 * mt + (lane & 1), cn = (lane >> 1) & 7;
    const int cb = (((cm & 7) | ((cn & 3) << 3)) << 3) | ((cm >> 3) << 2) | ((nt >> 4) << 1) | (cn >> 2);
    fout = (gu64*)(hbuf + WS_FLAG + (size_t)cb * 512 + (nt & 15) * 8);
  }
  if (wid == 0 && lane < 16) __hip_atomic_store(fout, MAGIC, __ATOMIC_RELAXED, __HIP_MEMORY_SCOPE_AGENT);
  auto wd_t0 = [&](int sg) __attribute__((always_inline)) -> int {
    return sg < 32 ? 7 * (sg >> 1) + 4 * (sg & 1) : sg < 48 ? 7 * (sg - 32) + 2 : 14 * (sg - 48) + 3;
  };
  auto wd_dt = [&](int sg) __attribute__((always_inline)) -> int { return sg < 32 ? 1 : sg < 48 ? 4 : 7; };
#pragma unroll
  for (int i = 0; i < 5; ++i) {
    const int t0 = wd_t0(2 * i + H);
    lds_u8* l = lds + (2 * i + H) * G2_STAGE;
    glds16(db + (size_t)t0 * (16 * D_MODEL * 2) + voffD, l + 8192 + r * 2048);
    glds16(db + (size_t)(t0 + 1) * (16 * D_MODEL * 2) + voffD, l + 8192 + r * 2048 + 1024);
  }
  unsigned pf3;
  {
    const int e2 = (wid < 7 ? wid : 6) * 64 + lane;
    const unsigned char* pw = wd + (size_t)(kh * 1792 + idx * 56 + (e2 >> 3)) * 2048 + (xcd & 1) * 1024 + (e2 & 7) * 128;
    asm volatile("global_load_dword %0, %1, off" : "=v"(pf3) : "v"(pw) : "memory");
  }
  u32x2 hs[4], hmx[2];
#pragma unroll
  for (int t = 0; t < 4; ++t) hs[t] = geglu4(accg[t][2], accu[t][2]);
#pragma unroll
  for (int t = 0; t < 4; t += 2) {
    f32x4 gg, uu;
#pragma unroll
    for (int rr = 0; rr < 4; ++rr) {
      auto sw = __builtin_amdgcn_permlane32_swap(__float_as_uint(accm[t][rr]), __float_as_uint(accm[t + 1][rr]), false, false);
      gg[rr] = __uint_as_float(sw[0]);
      uu[rr] = __uint_as_float(sw[1]);
    }
    hmx[t >> 1] = geglu4(gg, uu);
  }
  asm volatile("" : "+v"(hs[0]), "+v"(hs[1]), "+v"(hs[2]), "+v"(hs[3]), "+v"(hmx[0]), "+v"(hmx[1]));
  STAMP_NOW();
  auto wait_flags = [&](gu64* f) __attribute__((always_inline)) {
    const unsigned long long t0 = __builtin_amdgcn_s_memrealtime();
    for (;;) {
      const bool ok = __hip_atomic_load(f, __ATOMIC_RELAXED, __HIP_MEMORY_SCOPE_AGENT) == MAGIC;
      if (__all(ok)) break;
      if (__builtin_amdgcn_s_memrealtime() - t0 > 5000000ull) break;
      __builtin_amdgcn_s_sleep(1);
    }
  };
  if (wid == 0) {
    wait_flags(fin);
    __builtin_amdgcn_s_waitcnt(0xC07F);
  }
  asm volatile("" ::: "memory");
  BAR();
  asm volatile("" ::: "memory");
  STAMP_NOW();

#pragma unroll
  for (int i = 0; i < 5; ++i) {
    const int S = 2 * i + H;
    lds_u8* l = lds + S * G2_STAGE;
    glds16c(hb + (size_t)S * (128 * 1024) + voffH, l + r * 2048);
    glds16c(hb + (size_t)S * (128 * 1024) + 1024 + voffH, l + r * 2048 + 1024);
  }
  const unsigned hso = (unsigned)(((sbase + 32 + jj) * 128 + T0) * 1024 + wn * 512 + lane * 8);
  const unsigned hm = (unsigned)(((sbase + 48 + (jj >> 1)) * 128 + T0 + (lane >> 5)) * 1024 + (jj & 1) * 512 + ((2 * wn + (g & 1)) * 16 + tok) * 8);
  const int wm2 = r & 1, wn2 = r >> 1;
  const int hrd = wm2 * 4096 + lane * 16;
  unsigned drd[4];
  {
    const int f = q | ((g & 1) << 2);
#pragma unroll
    for (int u = 0; u < 4; ++u) drd[u] = lbase + H * G2_STAGE + 8192 + (8 * g + q) * 256 + (((4 * wn2 + u) ^ f) * 32) + 8 * p;
  }
  f32x4 acc[4][4];
#pragma unroll
  for (int t = 0; t < 4; ++t)
#pragma unroll
    for (int u = 0; u < 4; ++u) acc[t][u] = (f32x4){0.f, 0.f, 0.f, 0.f};
  const unsigned hra = lbase + H * G2_STAGE + hrd;
  const unsigned hra2 = lbase + H * G2_STAGE + wm2 * 4096 + lane * 8;
  WAIT_V(8);
  asm volatile("" ::"v"(pf3));
  BAR();
  if (wid == 0 && lane < 16) __hip_atomic_store(fin, 0ull, __ATOMIC_RELAXED, __HIP_MEMORY_SCOPE_AGENT);
  STAMP_NOW();
  bf16x4 dla[4], dha[4], dlb[4], dhb[4];
  bf16x8 ha[4], hb2[4];
  {
#pragma unroll
    for (int u = 0; u < 4; ++u) { dla[u] = tr_read<0>(drd[u]); dha[u] = tr_read<1024>(drd[u]); }
    ha[0] = ld128<0>(hra); ha[1] = ld128<1024>(hra); ha[2] = ld128<2048>(hra); ha[3] = ld128<3072>(hra);
  }
  int rs = 1, ws = 0;
  auto step2 = [&](int i, auto IL_, auto VM_, auto RD_, bf16x8(&hc)[4], bf16x8(&hn)[4], bf16x4(&dl)[4], bf16x4(&dh)[4], bf16x4(&dln)[4],
                  bf16x4(&dhn)[4]) __attribute__((always_inline)) {
    constexpr int IL = decltype(IL_)::value, VM = decltype(VM_)::value, RD = decltype(RD_)::value;
    const unsigned so = rs * (2 * G2_STAGE);
    const int S = 2 * (i + 5) + H;
    const int t0 = wd_t0(S), t1 = t0 + wd_dt(S);
    lds_u8* l = lds + ws * (2 * G2_STAGE) + H * G2_STAGE;
    constexpr int LL = IL;
    bf16x8 df;
    WAIT_L(0);
    if (RD) {
      WAIT_V(VM);
      BAR();
    }
    SCHED();
    df = cat8(dl[0], dh[0]);
    MF(acc[0][0], df, hc[0]);
    if (RD) dln[0] = tr_read<0>(drd[0] + so);
    SCHED();
    MF(acc[1][0], df, hc[1]);
    if (RD) dhn[0] = tr_read<1024>(drd[0] + so);
    SCHED();
    MF(acc[2][0], df, hc[2]);
    if (RD == 1) hn[0] = ld128<0>(hra + so);
    if (RD == 2) hn[0] = ld2x64<0>(hra2 + so);
    SCHED();
    MF(acc[3][0], df, hc[3]);
    if (RD == 1) hn[1] = ld128<1024>(hra + so);
    if (RD == 2) hn[1] = ld2x64<2>(hra2 + so);
    SCHED();
    df = cat8(dl[1], dh[1]);
    MF(acc[0][1], df, hc[0]);
    if (RD) dln[1] = tr_read<0>(drd[1] + so);
    SCHED();
    MF(acc[1][1], df, hc[1]);
    if (RD) dhn[1] = tr_read<1024>(drd[1] + so);
    SCHED();
    MF(acc[2][1], df, hc[2]);
    if (RD == 1) hn[2] = ld128<2048>(hra + so);
    if (RD == 2) hn[2] = ld2x64<4>(hra2 + so);
    SCHED();
    MF(acc[3][1], df, hc[3]);
    if (RD == 1) hn[3] = ld128<3072>(hra + so);
    if (RD == 2) hn[3] = ld2x64<6>(hra2 + so);
    SCHED();
    df = cat8(dl[2], dh[2]);
    MF(acc[0][2], df, hc[0]);
    if (RD) dln[2] = tr_read<0>(drd[2] + so);
    SCHED();
    MF(acc[1][2], df, hc[1]);
    if (RD) dhn[2] = tr_read<1024>(drd[2] + so);
    SCHED();
    MF(acc[2][2], df, hc[2]);
    if (RD) dln[3] = tr_read<0>(drd[3] + so);
    SCHED();
    MF(acc[3][2], df, hc[3]);
    if (RD) dhn[3] = tr_read<1024>(drd[3] + so);
    SCHED();
    df = cat8(dl[3], dh[3]);
    MF(acc[0][3], df, hc[0]);
    if (LL) glds16c(hb + (size_t)S * (128 * 1024) + voffH, l + r * 2048);
    SCHED();
    MF(acc[1][3], df, hc[1]);
    if (LL) glds16c(hb + (size_t)S * (128 * 1024) + 1024 + voffH, l + r * 2048 + 1024);
    SCHED();
    MF(acc[2][3], df, hc[2]);
    if (LL) glds16(db + (size_t)t0 * (16 * D_MODEL * 2) + voffD, l + 8192 + r * 2048);
    SCHED();
    MF(acc[3][3], df, hc[3]);
    if (LL) glds16(db + (size_t)t1 * (16 * D_MODEL * 2) + voffD, l + 8192 + r * 2048 + 1024);
    SCHED();
    rs = (rs == 4) ? 0 : rs + 1;
    ws = (ws == 4) ? 0 : ws + 1;
  };
#define G2_STEP_A(i, a, b, c) step2(i, IC<a>{}, IC<b>{}, IC<c>{}, ha, hb2, dla, dha, dlb, dhb)
#define G2_STEP_B(i, a, b, c) step2(i, IC<a>{}, IC<b>{}, IC<c>{}, hb2, ha, dlb, dhb, dla, dha)
  unsigned long long pv = 0;
  auto poll_issue = [&](gu64* f) __attribute__((always_inline)) {
    asm volatile("global_load_dwordx2 %0, %1, off sc1" : "+v"(pv) : "v"(f) : "memory");
  };
  auto poll_check = [&](gu64* f) __attribute__((always_inline)) {
    asm volatile("" : "+v"(pv));
    if (!__all(pv == MAGIC)) wait_flags(f);
    if (lane < 16) __hip_atomic_store(f, 0ull, __ATOMIC_RELAXED, __HIP_MEMORY_SCOPE_AGENT);
    __builtin_amdgcn_s_waitcnt(0xC07F);
  };
  G2_STEP_A(0, 1, 6, 1);
  __builtin_amdgcn_raw_buffer_store_b64(hs[0], rs_h, hso, 0, 16);
  __builtin_amdgcn_raw_buffer_store_b64(hs[1], rs_h, hso + 1024, 0, 16);
  G2_STEP_B(1, 1, 10, 1);
  __builtin_amdgcn_raw_buffer_store_b64(hs[2], rs_h, hso + 2048, 0, 16);
  __builtin_amdgcn_raw_buffer_store_b64(hs[3], rs_h, hso + 3072, 0, 16);
  G2_STEP_A(2, 1, 14, 1);
  __builtin_amdgcn_raw_buffer_store_b64(hmx[0], rs_h, hm, 0, 16);
  __builtin_amdgcn_raw_buffer_store_b64(hmx[1], rs_h, hm + 2048, 0, 16);
  STAMP_NOW();
  G2_STEP_B(3, 1, 18, 1);
  G2_STEP_A(4, 1, 18, 1);
  G2_STEP_B(5, 1, 16, 1);
  G2_STEP_A(6, 1, 14, 1);
  G2_STEP_B(7, 1, 12, 1);
  if (wid == 0 && lane < 16) __hip_atomic_store(fout + 16, MAGIC, __ATOMIC_RELAXED, __HIP_MEMORY_SCOPE_AGENT);
  G2_STEP_A(8, 1, 12, 1);
  G2_STEP_B(9, 1, 12, 1);
  if (wid == 0) poll_issue(fin + 16);
  G2_STEP_A(10, 1, 12, 1);
  STAMP_NOW();
  if (wid == 0) {
    WAIT_V(4);
    poll_check(fin + 16);
  }
  asm volatile("" ::: "memory");
  STAMP_NOW();
  G2_STEP_B(11, 1, 12, 1);
  G2_STEP_A(12, 1, 12, 1);
  G2_STEP_B(13, 1, 12, 1);
  G2_STEP_A(14, 1, 12, 1);
  G2_STEP_B(15, 1, 12, 2);
  for (int i = 16; i < 22; i += 2) {
    G2_STEP_A(i, 1, 12, 2);
    G2_STEP_B(i + 1, 1, 12, 2);
  }
  G2_STEP_A(22, 1, 12, 2);
  G2_STEP_B(23, 0, 12, 2);
  G2_STEP_A(24, 0, 8, 2);
  G2_STEP_B(25, 0, 4, 2);
  G2_STEP_A(26, 0, 0, 2);
  G2_STEP_B(27, 0, 0, 0);
  BAR();
  STAMP_NOW();

  f32x4 k0[4], k1[4];
  {
    lds_u8* dst = lds + ((1 - H) * 4 + r) * 8192 + lane * 16;
    if (H) {
#pragma unroll
      for (int u = 0; u < 4; ++u) {
        *(LAS f32x4*)(dst + u * 1024) = acc[0][u];
        *(LAS f32x4*)(dst + (4 + u) * 1024) = acc[1][u];
        k0[u] = acc[2][u]; k1[u] = acc[3][u];
      }
    } else {
#pragma unroll
      for (int u = 0; u < 4; ++u) {
        *(LAS f32x4*)(dst + u * 1024) = acc[2][u];
        *(LAS f32x4*)(dst + (4 + u) * 1024) = acc[3][u];
        k0[u] = acc[0][u]; k1[u] = acc[1][u];
      }
    }
  }
  __syncthreads();
  f32x4 keep[4], send[4];
  {
    lds_u8* src = lds + wid * 8192 + lane * 16;
#pragma unroll
    for (int u = 0; u < 4; ++u) {
      k0[u] += *(LAS f32x4*)(src + u * 1024);
      k1[u] += *(LAS f32x4*)(src + (4 + u) * 1024);
    }
    if (kh) {
#pragma unroll
      for (int u = 0; u < 4; ++u) { keep[u] = k1[u]; send[u] = k0[u]; }
    } else {
#pragma unroll
      for (int u = 0; u < 4; ++u) { keep[u] = k0[u]; send[u] = k1[u]; }
    }
  }
  STAMP_NOW();
  {
    const int pb = bid ^ 2;
    auto rs_x = __builtin_amdgcn_make_buffer_rsrc(hbuf + WS_XCH, 0, 256 * 32768, 0x00020000);
    const unsigned so = (unsigned)(pb * 32768 + wid * 4096 + lane * 16);
#pragma unroll
    for (int u = 0; u < 4; ++u) __builtin_amdgcn_raw_buffer_store_b128(__builtin_bit_cast(u32x4, send[u]), rs_x, so + u * 1024, 0, 16);
    WAIT_V(0);
    BAR();
    STAMP_NOW();
    if (wid == 0) {
      gu64* fl_out = (gu64*)(hbuf + WS_FLAG + (size_t)pb * 512 + 384);
      gu64* fl_in = (gu64*)(hbuf + WS_FLAG + (size_t)bid * 512 + 384);
      if (lane == 0) __hip_atomic_store(fl_out, MAGIC, __ATOMIC_RELAXED, __HIP_MEMORY_SCOPE_AGENT);
      const unsigned long long t0 = __builtin_amdgcn_s_memrealtime();
      for (;;) {
        const bool ok = __hip_atomic_load(fl_in, __ATOMIC_RELAXED, __HIP_MEMORY_SCOPE_AGENT) == MAGIC;
        if (__all(ok)) break;
        if (__builtin_amdgcn_s_memrealtime() - t0 > 5000000ull) break;
        __builtin_amdgcn_s_sleep(1);
      }
      if (lane == 0) __hip_atomic_store(fl_in, 0ull, __ATOMIC_RELAXED, __HIP_MEMORY_SCOPE_AGENT);
    }
    asm volatile("" ::: "memory");
    BAR();
    asm volatile("" ::: "memory");
    const unsigned si = (unsigned)(bid * 32768 + wid * 4096 + lane * 16);
#pragma unroll
    for (int u = 0; u < 4; ++u) keep[u] += __builtin_bit_cast(f32x4, __builtin_amdgcn_raw_buffer_load_b128(rs_x, si + u * 1024, 0, 16));
  }
  unsigned char* op = out + ((size_t)(mt2 * 128 + wm2 * 64 + (2 * H + kh) * 16 + tok) * D_MODEL + nt2 * 128 + wn2 * 64) * 2;
#pragma unroll
  for (int u = 0; u < 4; u += 2) {
    unsigned a0 = cvt_pk_bf16(keep[u][0], keep[u][1]), a1 = cvt_pk_bf16(keep[u][2], keep[u][3]);
    unsigned b0 = cvt_pk_bf16(keep[u + 1][0], keep[u + 1][1]), b1 = cvt_pk_bf16(keep[u + 1][2], keep[u + 1][3]);
    auto s0 = __builtin_amdgcn_permlane16_swap(a0, b0, false, false);
    auto s1 = __builtin_amdgcn_permlane16_swap(a1, b1, false, false);
    u32x4 v = {s0[0], s1[0], s0[1], s1[1]};
    const int col = (g & 1) ? (16 * (u + 1) + 4 * (g - 1)) : (16 * u + 4 * g);
    *(u32x4*)(op + col * 2) = v;
  }
  STAMP_NOW();
  STAMP_NOW();
  STAMP_OUT(hbuf + (64u << 20));
  }
}

extern "C" void kernel_launch(void* const* d_in, const int* in_sizes, int n_in, void* d_out, int out_size, void* d_ws,
                              size_t ws_size, hipStream_t stream) {
  const unsigned char* x = (const unsigned char*)d_in[0];
  const unsigned char* wg = (const unsigned char*)d_in[1];
  const unsigned char* wu = (const unsigned char*)d_in[2];
  const unsigned char* wd = (const unsigned char*)d_in[3];
  unsigned char* hbuf = (unsigned char*)d_ws;
  (void)hipFuncSetAttribute((const void*)k_fused, hipFuncAttributeMaxDynamicSharedMemorySize, G1_LDS);
  k_fused<<<256, 512, G1_LDS, stream>>>(x, wg, wu, hbuf, wd, (unsigned char*)d_out);
}
```

```cpp
#include <hip/hip_runtime.h>
#include <hip/hip_bf16.h>
#include <stdint.h>
#define PF3 1

typedef short bf16x8 __attribute__((ext_vector_type(8)));
typedef short bf16x4 __attribute__((ext_vector_type(4)));
typedef float f32x4 __attribute__((ext_vector_type(4)));
typedef float f32x2 __attribute__((ext_vector_type(2)));
typedef unsigned u32x2 __attribute__((ext_vector_type(2)));
typedef unsigned u32x4 __attribute__((ext_vector_type(4)));
#define LAS __attribute__((address_space(3)))
typedef LAS unsigned char lds_u8;

constexpr int M_TOK = 2048, D_MODEL = 1024, F_FF = 3584;
constexpr size_t WS_XCH = 16u << 20;
constexpr size_t WS_FLAG = 32u << 20;

#define WAIT_V(n) asm volatile("s_waitcnt vmcnt(%0)" ::"n"(n) : "memory")
#define WAIT_L0() asm volatile("s_waitcnt lgkmcnt(0)" ::: "memory")
#define BAR() __builtin_amdgcn_s_barrier()
#define SCHED() __builtin_amdgcn_sched_barrier(0)
#define FINL __device__ __forceinline__

#define STAMP_DECL
#define STAMP_NOW()
#define STAMP_OUT(base)
template <int N> struct IC { static constexpr int value = N; };

FINL void glds16c(const void* g, lds_u8* l) {
  __builtin_amdgcn_global_load_lds((const unsigned*)g, (LAS unsigned*)l, 16, 0, 16);
}
FINL void glds16(const void* g, lds_u8* l) {
  __builtin_amdgcn_global_load_lds((const unsigned*)g, (LAS unsigned*)l, 16, 0, 0);
}
template <int OFF> FINL bf16x4 tr_read(unsigned addr) {
  bf16x4 r;
  asm volatile("ds_read_b64_tr_b16 %0, %1 offset:%2" : "=v"(r) : "v"(addr), "n"(OFF) : "memory");
  return r;
}
template <int OFF> FINL bf16x8 ld128(unsigned addr) {
  bf16x8 r;
  asm volatile("ds_read_b128 %0, %1 offset:%2" : "=v"(r) : "v"(addr), "n"(OFF) : "memory");
  return r;
}
template <int O0> FINL bf16x8 ld2x64(unsigned addr) {
  bf16x8 r;
  asm volatile("ds_read2st64_b64 %0, %1 offset0:%2 offset1:%3" : "=v"(r) : "v"(addr), "n"(O0), "n"(O0 + 1) : "memory");
  return r;
}
#define WAIT_L(n) asm volatile("s_waitcnt lgkmcnt(%0)" ::"n"(n) : "memory")
FINL bf16x8 cat8(bf16x4 a, bf16x4 b) { return __builtin_shufflevector(a, b, 0, 1, 2, 3, 4, 5, 6, 7); }
FINL unsigned cvt_pk_bf16(float lo, float hi) {
  unsigned r;
  asm("v_cvt_pk_bf16_f32 %0, %1, %2" : "=v"(r) : "v"(lo), "v"(hi));
  return r;
}
FINL f32x2 geglu2(f32x2 g, f32x2 u) {
  const float c0 = -2.0f * 0.7978845608028654f * 1.4426950408889634f;
  const float c1 = c0 * 0.044715f;
  f32x2 t = g * g;
  f32x2 p = t * c1 + c0;
  f32x2 a = g * p;
  f32x2 e;
  e.x = __builtin_amdgcn_exp2f(a.x);
  e.y = __builtin_amdgcn_exp2f(a.y);
  f32x2 d = e + 1.0f;
  f32x2 r;
  r.x = __builtin_amdgcn_rcpf(d.x);
  r.y = __builtin_amdgcn_rcpf(d.y);
  return (g * r) * u;
}
FINL u32x2 geglu4(f32x4 g, f32x4 u) {
  f32x2 a = geglu2((f32x2){g[0], g[1]}, (f32x2){u[0], u[1]});
  f32x2 b = geglu2((f32x2){g[2], g[3]}, (f32x2){u[2], u[3]});
  u32x2 o;
  o.x = cvt_pk_bf16(a.x, a.y);
  o.y = cvt_pk_bf16(b.x, b.y);
  return o;
}

constexpr int G1_WGRP = 2048;
constexpr int G1_WSLOT = 8 * G1_WGRP;
constexpr int G1_NW = 4;
constexpr int G1_XSLOT = 32768;
constexpr int G1_NX = 3;
constexpr int G1_XOFF = G1_NW * G1_WSLOT;
constexpr int G1_LDS = G1_XOFF + G1_NX * G1_XSLOT;

constexpr int G2_STAGE = 16384;
constexpr unsigned long long MAGIC = 0x5E1F0A3C97B2D461ull;
typedef __attribute__((address_space(1))) unsigned long long gu64;

__global__ __launch_bounds__(512, 2) void k_fused(const unsigned char* __restrict__ x, const unsigned char* __restrict__ wg,
                                                  const unsigned char* __restrict__ wu, unsigned char* hbuf,
                                                  const unsigned char* __restrict__ wd, unsigned char* __restrict__ out) {
  extern __shared__ __attribute__((aligned(16))) unsigned char smem_raw[];
  lds_u8* lds = (lds_u8*)smem_raw;
  const int tid = threadIdx.x;
  const int wid = __builtin_amdgcn_readfirstlane(tid >> 6);
  const int lane = tid & 63;
  const int half = wid >> 2, wm = wid & 3, wn = wid >> 2;
  STAMP_DECL
  STAMP_NOW();
  const int bid = blockIdx.x;
  const int xcd = bid & 7, idx = bid >> 3;
  const int mt = (xcd >> 2) * 4 + (idx & 3);
  const int nt = (xcd & 3) * 8 + (idx >> 2);
  const int g = lane >> 4, q = (lane >> 2) & 3, p = lane & 3, tok = lane & 15;

  int li = lane - ((wid >> 1) & 1) * 8;
  li = (li < 0 || li >= 56) ? 0 : li;
  const unsigned voffW = (unsigned)((4 * wid + li / 14) * (F_FF * 2) + (li % 14) * 16);
  unsigned voffX[2];
#pragma unroll
  for (int i = 0; i < 2; ++i)
    voffX[i] = (unsigned)((8 * (2 * wid + i) + (lane >> 3)) * (D_MODEL * 2) + (((lane & 7) ^ ((i << 2) | (lane >> 4))) * 16));
  const unsigned char* wgb = wg + (size_t)nt * 224;
  const unsigned char* wub = wu + (size_t)nt * 224;
  const unsigned char* xb = x + (size_t)mt * 256 * (D_MODEL * 2);
  const int wdst = wid * G1_WGRP;
  const int xdst = G1_XOFF + wid * 2048;

  const int rot2 = 0;
  auto stageW = [&](int j, int slot) __attribute__((always_inline)) {
    const size_t go = (size_t)((j + 2 * rot2) & 31) * (32 * F_FF * 2);
    glds16(wgb + go + voffW, lds + slot * G1_WSLOT + wdst);
    glds16(wub + go + voffW, lds + slot * G1_WSLOT + wdst + 1024);
  };
  auto stageX = [&](int t, int hb, int slot) __attribute__((always_inline)) {
    const size_t go = (size_t)hb * (128 * D_MODEL * 2) + (size_t)((t + rot2) & 15) * 128;
    glds16(xb + go + voffX[0], lds + xdst + slot * G1_XSLOT + hb * 16384);
    glds16(xb + go + voffX[1], lds + xdst + slot * G1_XSLOT + hb * 16384 + 1024);
  };

  const unsigned lbase = (unsigned)(size_t)lds;
  const unsigned wrd = lbase + 2 * g * G1_WGRP + (g & 1) * 128 + q * 224 + p * 8 + wn * 128;
  const unsigned wrdm = lbase + 2 * g * G1_WGRP + (g & 1) * 128 + q * 224 + 96 + wn * 16 + (p & 1) * 8 + (p >> 1) * 1024;
  int xrd[2];
#pragma unroll
  for (int s = 0; s < 2; ++s) xrd[s] = G1_XOFF + (wm * 64 + tok) * 128 + ((((s << 2) | g) ^ ((tok >> 1) & 7)) * 16);

  f32x4 accg[4][3], accu[4][3], accm[4];
#pragma unroll
  for (int t = 0; t < 4; ++t) {
#pragma unroll
    for (int u = 0; u < 3; ++u) { accg[t][u] = (f32x4){0.f, 0.f, 0.f, 0.f}; accu[t][u] = (f32x4){0.f, 0.f, 0.f, 0.f}; }
    accm[t] = (f32x4){0.f, 0.f, 0.f, 0.f};
  }

  const int H = wid >> 2, r = wid & 3;
  const int kh = (xcd >> 1) & 1;
  const int mt2 = (xcd >> 2) * 8 + (idx & 7);
  const int nt2 = (xcd & 1) * 4 + (idx >> 3);
  const unsigned char* hb = hbuf + ((size_t)kh * 56 * 128 + mt2 * 8 + 2 * r) * 1024;
  const unsigned char* db = wd + (size_t)kh * 56 * 32 * (D_MODEL * 2) + (size_t)nt2 * 256;
  unsigned cpf = 0;
  {
    const unsigned char* pc = (const unsigned char*)__builtin_amdgcn_s_getpc();
    const unsigned char* pa = pc + (size_t)((wid >= 5 ? wid - 5 : 0) * 64 + lane) * 128;
    asm volatile("global_load_dword %0, %1, off" : "+v"(cpf) : "v"(pa) : "memory");
  }
  stageX(0, 0, 0); stageX(0, 1, 0); stageW(0, 0);
  stageW(1, 1); stageX(1, 0, 1);
  stageW(2, 2); stageX(1, 1, 1);
  stageW(3, 3); stageX(2, 0, 2);
  WAIT_V(12);
  asm volatile("" ::"v"(cpf));
  BAR();
  STAMP_NOW();

  const unsigned xra[2] = {lbase + (unsigned)xrd[0], lbase + (unsigned)xrd[1]};
  bf16x4 wl[7], wh[7];
  bf16x8 xa[4], xb2[4];
  {
    wl[0] = tr_read<0>(wrd); wh[0] = tr_read<G1_WGRP>(wrd);
    wl[1] = tr_read<32>(wrd); wh[1] = tr_read<32 + G1_WGRP>(wrd);
    wl[2] = tr_read<64>(wrd); wh[2] = tr_read<64 + G1_WGRP>(wrd);
    wl[3] = tr_read<1024>(wrd); wh[3] = tr_read<1024 + G1_WGRP>(wrd);
    wl[4] = tr_read<1024 + 32>(wrd); wh[4] = tr_read<1024 + 32 + G1_WGRP>(wrd);
    wl[5] = tr_read<1024 + 64>(wrd); wh[5] = tr_read<1024 + 64 + G1_WGRP>(wrd);
    wl[6] = tr_read<0>(wrdm); wh[6] = tr_read<G1_WGRP>(wrdm);
    xa[0] = ld128<0>(xra[0]); xa[1] = ld128<2048>(xra[0]); xa[2] = ld128<4096>(xra[0]); xa[3] = ld128<6144>(xra[0]);
    WAIT_L(0);
  }
  int xs_rd = 0, xs_wr = 2;
  auto step = [&](int k, auto S_, auto IW_, auto IX_, auto VM_, auto RD_, bf16x8(&xc)[4], bf16x8(&xn)[4], auto IP_) __attribute__((always_inline)) {
    constexpr int S = decltype(S_)::value;
    constexpr int IP = decltype(IP_)::value;
    constexpr int IW = decltype(IW_)::value, IX = decltype(IX_)::value, VM = decltype(VM_)::value, RD = decltype(RD_)::value;
    if (S) xs_rd = (xs_rd == 2) ? 0 : xs_rd + 1;
    const unsigned wb = ((k + 1) & 3) * G1_WSLOT + wrd;
    const unsigned wbm = ((k + 1) & 3) * G1_WSLOT + wrdm;
    const unsigned xbp = xs_rd * G1_XSLOT + xra[S ^ 1];
#define MF(ACC, WF, XF) ACC = __builtin_amdgcn_mfma_f32_16x16x32_bf16(WF, XF, ACC, 0, 0, 0); SCHED();
    constexpr int LW = IW, LX = IX;
    const size_t goW = (size_t)((k + 4 + 2 * rot2) & 31) * (32 * F_FF * 2);
    const size_t goX = (size_t)(S ^ 1) * (128 * D_MODEL * 2) + (size_t)((((k + 1) >> 1) + 2 + rot2) & 15) * 128;
    lds_u8* ldW = lds + (k & 3) * G1_WSLOT + wdst;
    lds_u8* ldX = lds + xdst + xs_wr * G1_XSLOT + (S ^ 1) * 16384;
    bf16x8 wf;
    WAIT_L(10);
    SCHED();
    wf = cat8(wl[0], wh[0]);
    MF(accg[0][0], wf, xc[0]);
    MF(accg[1][0], wf, xc[1]);
    MF(accg[2][0], wf, xc[2]);
    MF(accg[3][0], wf, xc[3]);
    WAIT_L(0);
    if (RD) {
      if (VM >= 0) WAIT_V(VM);
      BAR();
    }
    SCHED();
    wf = cat8(wl[3], wh[3]);
    MF(accu[0][0], wf, xc[0]);
    if (RD) wl[0] = tr_read<0>(wb);
    SCHED();
    MF(accu[1][0], wf, xc[1]);
    if (RD) wh[0] = tr_read<0 + G1_WGRP>(wb);
    SCHED();
    MF(accu[2][0], wf, xc[2]);
    if (RD) xn[0] = ld128<0>(xbp);
    SCHED();
    MF(accu[3][0], wf, xc[3]);
    if (RD) xn[1] = ld128<2048>(xbp);
    SCHED();
    wf = cat8(wl[1], wh[1]);
    MF(accg[0][1], wf, xc[0]);
    if (RD) wl[3] = tr_read<1024>(wb);
    SCHED();
    MF(accg[1][1], wf, xc[1]);
    if (RD) wh[3] = tr_read<1024 + G1_WGRP>(wb);
    SCHED();
    MF(accg[2][1], wf, xc[2]);
    if (RD) xn[2] = ld128<4096>(xbp);
    SCHED();
    MF(accg[3][1], wf, xc[3]);
    if (RD) xn[3] = ld128<6144>(xbp);
    SCHED();
    wf = cat8(wl[4], wh[4]);
    MF(accu[0][1], wf, xc[0]);
    if (RD) wl[1] = tr_read<32>(wb);
    SCHED();
    MF(accu[1][1], wf, xc[1]);
    if (RD) wh[1] = tr_read<32 + G1_WGRP>(wb);
    SCHED();
    MF(accu[2][1], wf, xc[2]);
    if (LW) glds16(wgb + goW + voffW, ldW);
    SCHED();
    MF(accu[3][1], wf, xc[3]);
    wf = cat8(wl[2], wh[2]);
    MF(accg[0][2], wf, xc[0]);
    if (RD) wl[4] = tr_read<1024 + 32>(wb);
    SCHED();
    MF(accg[1][2], wf, xc[1]);
    if (RD) wh[4] = tr_read<1024 + 32 + G1_WGRP>(wb);
    SCHED();
    MF(accg[2][2], wf, xc[2]);
    if (LW) glds16(wub + goW + voffW, ldW + 1024);
    SCHED();
    MF(accg[3][2], wf, xc[3]);
    wf = cat8(wl[5], wh[5]);
    MF(accu[0][2], wf, xc[0]);
    if (RD) wl[2] = tr_read<64>(wb);
    SCHED();
    MF(accu[1][2], wf, xc[1]);
    if (RD) wh[2] = tr_read<64 + G1_WGRP>(wb);
    SCHED();
    MF(accu[2][2], wf, xc[2]);
    if (LX) glds16(xb + goX + voffX[0], ldX);
    SCHED();
    MF(accu[3][2], wf, xc[3]);
    wf = cat8(wl[6], wh[6]);
    MF(accm[0], wf, xc[0]);
    if (RD) wl[5] = tr_read<1024 + 64>(wb);
    SCHED();
    MF(accm[1], wf, xc[1]);
    if (RD) wh[5] = tr_read<1024 + 64 + G1_WGRP>(wb);
    SCHED();
    MF(accm[2], wf, xc[2]);
    if (LX) glds16(xb + goX + voffX[1], ldX + 1024);
    SCHED();
    MF(accm[3], wf, xc[3]);
    if (RD) wl[6] = tr_read<0>(wbm);
    if (RD) wh[6] = tr_read<0 + G1_WGRP>(wbm);
    SCHED();
    if (!S) xs_wr = (xs_wr == 2) ? 0 : xs_wr + 1;
  };
  for (int k = 0; k < 26; k += 2) {
    step(k, IC<0>{}, IC<1>{}, IC<1>{}, IC<8>{}, IC<1>{}, xa, xb2, IC<0>{});
    step(k + 1, IC<1>{}, IC<1>{}, IC<1>{}, IC<8>{}, IC<1>{}, xb2, xa, IC<0>{});
  }
  step(26, IC<0>{}, IC<1>{}, IC<1>{}, IC<8>{}, IC<1>{}, xa, xb2, IC<0>{});
  step(27, IC<1>{}, IC<1>{}, IC<0>{}, IC<8>{}, IC<1>{}, xb2, xa, IC<0>{});
  {
    bf16x8 wf;
    WAIT_L(0);
    WAIT_V(6);
    BAR();
    SCHED();
    wf = cat8(wl[0], wh[0]);
    MF(accg[0][0], wf, xa[0]);
    xb2[0] = ld128<0>((xra[1] + 2 * G1_XSLOT));
    SCHED();
    MF(accg[1][0], wf, xa[1]);
    xb2[1] = ld128<2048>((xra[1] + 2 * G1_XSLOT));
    SCHED();
    MF(accg[2][0], wf, xa[2]);
    xb2[2] = ld128<4096>((xra[1] + 2 * G1_XSLOT));
    SCHED();
    MF(accg[3][0], wf, xa[3]);
    xb2[3] = ld128<6144>((xra[1] + 2 * G1_XSLOT));
    SCHED();
    wf = cat8(wl[3], wh[3]);
    MF(accu[0][0], wf, xa[0]);
    wl[0] = tr_read<0>((wrd + 1 * G1_WSLOT));
    SCHED();
    MF(accu[1][0], wf, xa[1]);
    wh[0] = tr_read<0 + G1_WGRP>((wrd + 1 * G1_WSLOT));
    SCHED();
    MF(accu[2][0], wf, xa[2]);
    MF(accu[3][0], wf, xa[3]);
    wf = cat8(wl[1], wh[1]);
    MF(accg[0][1], wf, xa[0]);
    wl[3] = tr_read<1024>((wrd + 1 * G1_WSLOT));
    SCHED();
    MF(accg[1][1], wf, xa[1]);
    wh[3] = tr_read<1024 + G1_WGRP>((wrd + 1 * G1_WSLOT));
    SCHED();
    MF(accg[2][1], wf, xa[2]);
    MF(accg[3][1], wf, xa[3]);
    wf = cat8(wl[4], wh[4]);
    MF(accu[0][1], wf, xa[0]);
    wl[1] = tr_read<32>((wrd + 1 * G1_WSLOT));
    SCHED();
    MF(accu[1][1], wf, xa[1]);
    wh[1] = tr_read<32 + G1_WGRP>((wrd + 1 * G1_WSLOT));
    SCHED();
    MF(accu[2][1], wf, xa[2]);
    MF(accu[3][1], wf, xa[3]);
    wl[4] = tr_read<1024 + 32>((wrd + 1 * G1_WSLOT));
    wh[4] = tr_read<1024 + 32 + G1_WGRP>((wrd + 1 * G1_WSLOT));
    SCHED();
  }
  {
    bf16x8 wf;
    WAIT_L(0);
    WAIT_V(2);
    BAR();
    SCHED();
    wf = cat8(wl[0], wh[0]);
    MF(accg[0][0], wf, xb2[0]);
    xa[0] = ld128<0>((xra[0] + 0 * G1_XSLOT));
    SCHED();
    MF(accg[1][0], wf, xb2[1]);
    xa[1] = ld128<2048>((xra[0] + 0 * G1_XSLOT));
    SCHED();
    MF(accg[2][0], wf, xb2[2]);
    xa[2] = ld128<4096>((xra[0] + 0 * G1_XSLOT));
    SCHED();
    MF(accg[3][0], wf, xb2[3]);
    xa[3] = ld128<6144>((xra[0] + 0 * G1_XSLOT));
    SCHED();
    wf = cat8(wl[3], wh[3]);
    MF(accu[0][0], wf, xb2[0]);
    wl[0] = tr_read<0>((wrd + 2 * G1_WSLOT));
    SCHED();
    MF(accu[1][0], wf, xb2[1]);
    wh[0] = tr_read<0 + G1_WGRP>((wrd + 2 * G1_WSLOT));
    SCHED();
    MF(accu[2][0], wf, xb2[2]);
    MF(accu[3][0], wf, xb2[3]);
    wf = cat8(wl[1], wh[1]);
    MF(accg[0][1], wf, xb2[0]);
    wl[3] = tr_read<1024>((wrd + 2 * G1_WSLOT));
    SCHED();
    MF(accg[1][1], wf, xb2[1]);
    wh[3] = tr_read<1024 + G1_WGRP>((wrd + 2 * G1_WSLOT));
    SCHED();
    MF(accg[2][1], wf, xb2[2]);
    MF(accg[3][1], wf, xb2[3]);
    wf = cat8(wl[4], wh[4]);
    MF(accu[0][1], wf, xb2[0]);
    wl[1] = tr_read<32>((wrd + 2 * G1_WSLOT));
    SCHED();
    MF(accu[1][1], wf, xb2[1]);
    wh[1] = tr_read<32 + G1_WGRP>((wrd + 2 * G1_WSLOT));
    SCHED();
    MF(accu[2][1], wf, xb2[2]);
    MF(accu[3][1], wf, xb2[3]);
    wl[4] = tr_read<1024 + 32>((wrd + 2 * G1_WSLOT));
    wh[4] = tr_read<1024 + 32 + G1_WGRP>((wrd + 2 * G1_WSLOT));
    SCHED();
  }
  {
    bf16x8 wf;
    WAIT_L(0);
    WAIT_V(0);
    BAR();
    SCHED();
    wf = cat8(wl[0], wh[0]);
    MF(accg[0][0], wf, xa[0]);
    xb2[0] = ld128<0>((xra[1] + 0 * G1_XSLOT));
    SCHED();
    MF(accg[1][0], wf, xa[1]);
    xb2[1] = ld128<2048>((xra[1] + 0 * G1_XSLOT));
    SCHED();
    MF(accg[2][0], wf, xa[2]);
    xb2[2] = ld128<4096>((xra[1] + 0 * G1_XSLOT));
    SCHED();
    MF(accg[3][0], wf, xa[3]);
    xb2[3] = ld128<6144>((xra[1] + 0 * G1_XSLOT));
    SCHED();
    wf = cat8(wl[3], wh[3]);
    MF(accu[0][0], wf, xa[0]);
    wl[0] = tr_read<0>((wrd + 3 * G1_WSLOT));
    SCHED();
    MF(accu[1][0], wf, xa[1]);
    wh[0] = tr_read<0 + G1_WGRP>((wrd + 3 * G1_WSLOT));
    SCHED();
    MF(accu[2][0], wf, xa[2]);
    MF(accu[3][0], wf, xa[3]);
    wf = cat8(wl[1], wh[1]);
    MF(accg[0][1], wf, xa[0]);
    wl[3] = tr_read<1024>((wrd + 3 * G1_WSLOT));
    SCHED();
    MF(accg[1][1], wf, xa[1]);
    wh[3] = tr_read<1024 + G1_WGRP>((wrd + 3 * G1_WSLOT));
    SCHED();
    MF(accg[2][1], wf, xa[2]);
    MF(accg[3][1], wf, xa[3]);
    wf = cat8(wl[4], wh[4]);
    MF(accu[0][1], wf, xa[0]);
    wl[1] = tr_read<32>((wrd + 3 * G1_WSLOT));
    SCHED();
    MF(accu[1][1], wf, xa[1]);
    wh[1] = tr_read<32 + G1_WGRP>((wrd + 3 * G1_WSLOT));
    SCHED();
    MF(accu[2][1], wf, xa[2]);
    MF(accu[3][1], wf, xa[3]);
    wl[4] = tr_read<1024 + 32>((wrd + 3 * G1_WSLOT));
    wh[4] = tr_read<1024 + 32 + G1_WGRP>((wrd + 3 * G1_WSLOT));
    SCHED();
  }
  unsigned pf3;
  {
    const int e2 = (wid < 7 ? wid : 6) * 64 + lane;
    const unsigned char* pw = wd + (size_t)(kh * 1792 + idx * 56 + (e2 >> 3)) * 2048 + (xcd & 1) * 1024 + (e2 & 7) * 128;
    asm volatile("global_load_dword %0, %1, off" : "=v"(pf3) : "v"(pw) : "memory");
  }
  {
    bf16x8 wf;
    WAIT_L(0);
    SCHED();
    wf = cat8(wl[0], wh[0]);
    MF(accg[0][0], wf, xb2[0]);
    xa[0] = ld128<0>((xra[0] + 2 * G1_XSLOT));
    SCHED();
    MF(accg[1][0], wf, xb2[1]);
    xa[1] = ld128<2048>((xra[0] + 2 * G1_XSLOT));
    SCHED();
    MF(accg[2][0], wf, xb2[2]);
    xa[2] = ld128<4096>((xra[0] + 2 * G1_XSLOT));
    SCHED();
    MF(accg[3][0], wf, xb2[3]);
    xa[3] = ld128<6144>((xra[0] + 2 * G1_XSLOT));
    SCHED();
    wf = cat8(wl[3], wh[3]);
    MF(accu[0][0], wf, xb2[0]);
    MF(accu[1][0], wf, xb2[1]);
    MF(accu[2][0], wf, xb2[2]);
    MF(accu[3][0], wf, xb2[3]);
    wf = cat8(wl[1], wh[1]);
    MF(accg[0][1], wf, xb2[0]);
    MF(accg[1][1], wf, xb2[1]);
    MF(accg[2][1], wf, xb2[2]);
    MF(accg[3][1], wf, xb2[3]);
    wf = cat8(wl[4], wh[4]);
    MF(accu[0][1], wf, xb2[0]);
    MF(accu[1][1], wf, xb2[1]);
    MF(accu[2][1], wf, xb2[2]);
    MF(accu[3][1], wf, xb2[3]);
    SCHED();
  }
  STAMP_NOW();
  auto rs_h = __builtin_amdgcn_make_buffer_rsrc(hbuf, 0, 112 * 128 * 1024, 0x00020000);
  const int T0 = mt * 16 + wm * 4;
  const int sbase = (nt >> 4) * 56, jj = nt & 15;
  {
    const unsigned ho = (unsigned)(((sbase + 2 * jj + wn) * 128 + T0) * 1024 + lane * 16);
#pragma unroll
    for (int t = 0; t < 4; ++t) {
      const u32x2 a = geglu4(accg[t][0], accu[t][0]), b = geglu4(accg[t][1], accu[t][1]);
      __builtin_amdgcn_raw_buffer_store_b128((u32x4){a.x, a.y, b.x, b.y}, rs_h, ho + t * 1024, 0, 16);
    }
  }
  SCHED();
  STAMP_NOW();
  {
    bf16x8 wf;
    WAIT_L(0);
    SCHED();
    wf = cat8(wl[2], wh[2]);
    MF(accg[0][2], wf, xa[0]);
    xb2[0] = ld128<0>((xra[1] + 2 * G1_XSLOT));
    SCHED();
    MF(accg[1][2], wf, xa[1]);
    xb2[1] = ld128<2048>((xra[1] + 2 * G1_XSLOT));
    SCHED();
    MF(accg[2][2], wf, xa[2]);
    xb2[2] = ld128<4096>((xra[1] + 2 * G1_XSLOT));
    SCHED();
    MF(accg[3][2], wf, xa[3]);
    xb2[3] = ld128<6144>((xra[1] + 2 * G1_XSLOT));
    SCHED();
    wf = cat8(wl[5], wh[5]);
    MF(accu[0][2], wf, xa[0]);
    wl[2] = tr_read<64>((wrd + 1 * G1_WSLOT));
    SCHED();
    MF(accu[1][2], wf, xa[1]);
    wh[2] = tr_read<64 + G1_WGRP>((wrd + 1 * G1_WSLOT));
    SCHED();
    MF(accu[2][2], wf, xa[2]);
    MF(accu[3][2], wf, xa[3]);
    wf = cat8(wl[6], wh[6]);
    MF(accm[0], wf, xa[0]);
    wl[5] = tr_read<1024 + 64>((wrd + 1 * G1_WSLOT));
    SCHED();
    MF(accm[1], wf, xa[1]);
    wh[5] = tr_read<1024 + 64 + G1_WGRP>((wrd + 1 * G1_WSLOT));
    SCHED();
    MF(accm[2], wf, xa[2]);
    MF(accm[3], wf, xa[3]);
    wl[6] = tr_read<0>((wrdm + 1 * G1_WSLOT));
    wh[6] = tr_read<0 + G1_WGRP>((wrdm + 1 * G1_WSLOT));
    SCHED();
  }
  {
    bf16x8 wf;
    WAIT_L(0);
    SCHED();
    wf = cat8(wl[2], wh[2]);
    MF(accg[0][2], wf, xb2[0]);
    xa[0] = ld128<0>((xra[0] + 0 * G1_XSLOT));
    SCHED();
    MF(accg[1][2], wf, xb2[1]);
    xa[1] = ld128<2048>((xra[0] + 0 * G1_XSLOT));
    SCHED();
    MF(accg[2][2], wf, xb2[2]);
    xa[2] = ld128<4096>((xra[0] + 0 * G1_XSLOT));
    SCHED();
    MF(accg[3][2], wf, xb2[3]);
    xa[3] = ld128<6144>((xra[0] + 0 * G1_XSLOT));
    SCHED();
    wf = cat8(wl[5], wh[5]);
    MF(accu[0][2], wf, xb2[0]);
    wl[2] = tr_read<64>((wrd + 2 * G1_WSLOT));
    SCHED();
    MF(accu[1][2], wf, xb2[1]);
    wh[2] = tr_read<64 + G1_WGRP>((wrd + 2 * G1_WSLOT));
    SCHED();
    MF(accu[2][2], wf, xb2[2]);
    MF(accu[3][2], wf, xb2[3]);
    wf = cat8(wl[6], wh[6]);
    MF(accm[0], wf, xb2[0]);
    wl[5] = tr_read<1024 + 64>((wrd + 2 * G1_WSLOT));
    SCHED();
    MF(accm[1], wf, xb2[1]);
    wh[5] = tr_read<1024 + 64 + G1_WGRP>((wrd + 2 * G1_WSLOT));
    SCHED();
    MF(accm[2], wf, xb2[2]);
    MF(accm[3], wf, xb2[3]);
    wl[6] = tr_read<0>((wrdm + 2 * G1_WSLOT));
    wh[6] = tr_read<0 + G1_WGRP>((wrdm + 2 * G1_WSLOT));
    SCHED();
  }
  {
    bf16x8 wf;
    WAIT_L(0);
    SCHED();
    wf = cat8(wl[2], wh[2]);
    MF(accg[0][2], wf, xa[0]);
    xb2[0] = ld128<0>((xra[1] + 0 * G1_XSLOT));
    SCHED();
    MF(accg[1][2], wf, xa[1]);
    xb2[1] = ld128<2048>((xra[1] + 0 * G1_XSLOT));
    SCHED();
    MF(accg[2][2], wf, xa[2]);
    xb2[2] = ld128<4096>((xra[1] + 0 * G1_XSLOT));
    SCHED();
    MF(accg[3][2], wf, xa[3]);
    xb2[3] = ld128<6144>((xra[1] + 0 * G1_XSLOT));
    SCHED();
    wf = cat8(wl[5], wh[5]);
    MF(accu[0][2], wf, xa[0]);
    wl[2] = tr_read<64>((wrd + 3 * G1_WSLOT));
    SCHED();
    MF(accu[1][2], wf, xa[1]);
    wh[2] = tr_read<64 + G1_WGRP>((wrd + 3 * G1_WSLOT));
    SCHED();
    MF(accu[2][2], wf, xa[2]);
    MF(accu[3][2], wf, xa[3]);
    wf = cat8(wl[6], wh[6]);
    MF(accm[0], wf, xa[0]);
    wl[5] = tr_read<1024 + 64>((wrd + 3 * G1_WSLOT));
    SCHED();
    MF(accm[1], wf, xa[1]);
    wh[5] = tr_read<1024 + 64 + G1_WGRP>((wrd + 3 * G1_WSLOT));
    SCHED();
    MF(accm[2], wf, xa[2]);
    MF(accm[3], wf, xa[3]);
    wl[6] = tr_read<0>((wrdm + 3 * G1_WSLOT));
    wh[6] = tr_read<0 + G1_WGRP>((wrdm + 3 * G1_WSLOT));
    SCHED();
  }
  {
    bf16x8 wf;
    WAIT_L(0);
    SCHED();
    wf = cat8(wl[2], wh[2]);
    MF(accg[0][2], wf, xb2[0]);
    MF(accg[1][2], wf, xb2[1]);
    MF(accg[2][2], wf, xb2[2]);
    MF(accg[3][2], wf, xb2[3]);
    wf = cat8(wl[5], wh[5]);
    MF(accu[0][2], wf, xb2[0]);
    MF(accu[1][2], wf, xb2[1]);
    MF(accu[2][2], wf, xb2[2]);
    MF(accu[3][2], wf, xb2[3]);
    wf = cat8(wl[6], wh[6]);
    MF(accm[0], wf, xb2[0]);
    MF(accm[1], wf, xb2[1]);
    MF(accm[2], wf, xb2[2]);
    MF(accm[3], wf, xb2[3]);
    SCHED();
  }
  WAIT_V(0);
  BAR();
  STAMP_NOW();
  const unsigned voffH = (unsigned)(lane * 16);
  const unsigned voffD = (unsigned)((4 * r + g) * (D_MODEL * 2) + ((((tok >> 1) ^ (g | ((r & 1) << 2))) << 1) | (lane & 1)) * 16);
  gu64* fout;
  gu64* fin = (gu64*)(hbuf + WS_FLAG + (size_t)bid * 512 + (lane & 15) * 8);
  {
    const int cm = 2 * mt + (lane & 1), cn = (lane >> 1) & 7;
    const int cb = (((cm & 7) | ((cn & 3) << 3)) << 3) | ((cm >> 3) << 2) | ((nt >> 4) << 1) | (cn >> 2);
    fout = (gu64*)(hbuf + WS_FLAG + (size_t)cb * 512 + (nt & 15) * 8);
  }
  if (wid == 0 && lane < 16) __hip_atomic_store(fout, MAGIC, __ATOMIC_RELAXED, __HIP_MEMORY_SCOPE_AGENT);
  auto wd_t0 = [&](int sg) __attribute__((always_inline)) -> int {
    return sg < 32 ? 7 * (sg >> 1) + 4 * (sg & 1) : sg < 48 ? 7 * (sg - 32) + 2 : 14 * (sg - 48) + 3;
  };
  auto wd_dt = [&](int sg) __attribute__((always_inline)) -> int { return sg < 32 ? 1 : sg < 48 ? 4 : 7; };
#pragma unroll
  for (int i = 0; i < 5; ++i) {
    const int t0 = wd_t0(2 * i + H);
    lds_u8* l = lds + (2 * i + H) * G2_STAGE;
    glds16(db + (size_t)t0 * (16 * D_MODEL * 2) + voffD, l + 8192 + r * 2048);
    glds16(db + (size_t)(t0 + 1) * (16 * D_MODEL * 2) + voffD, l + 8192 + r * 2048 + 1024);
  }
  u32x2 hs[4], hmx[2];
#pragma unroll
  for (int t = 0; t < 4; ++t) hs[t] = geglu4(accg[t][2], accu[t][2]);
#pragma unroll
  for (int t = 0; t < 4; t += 2) {
    f32x4 gg, uu;
#pragma unroll
    for (int rr = 0; rr < 4; ++rr) {
      auto sw = __builtin_amdgcn_permlane32_swap(__float_as_uint(accm[t][rr]), __float_as_uint(accm[t + 1][rr]), false, false);
      gg[rr] = __uint_as_float(sw[0]);
      uu[rr] = __uint_as_float(sw[1]);
    }
    hmx[t >> 1] = geglu4(gg, uu);
  }
  asm volatile("" : "+v"(hs[0]), "+v"(hs[1]), "+v"(hs[2]), "+v"(hs[3]), "+v"(hmx[0]), "+v"(hmx[1]));
  STAMP_NOW();
  auto wait_flags = [&](gu64* f) __attribute__((always_inline)) {
    const unsigned long long t0 = __builtin_amdgcn_s_memrealtime();
    for (;;) {
      const bool ok = __hip_atomic_load(f, __ATOMIC_RELAXED, __HIP_MEMORY_SCOPE_AGENT) == MAGIC;
      if (__all(ok)) break;
      if (__builtin_amdgcn_s_memrealtime() - t0 > 5000000ull) break;
      __builtin_amdgcn_s_sleep(1);
    }
  };
  if (wid == 0) {
    wait_flags(fin);
    __builtin_amdgcn_s_waitcnt(0xC07F);
  }
  asm volatile("" ::: "memory");
  BAR();
  asm volatile("" ::: "memory");
  STAMP_NOW();

#pragma unroll
  for (int i = 0; i < 5; ++i) {
    const int S = 2 * i + H;
    lds_u8* l = lds + S * G2_STAGE;
    glds16c(hb + (size_t)S * (128 * 1024) + voffH, l + r * 2048);
    glds16c(hb + (size_t)S * (128 * 1024) + 1024 + voffH, l + r * 2048 + 1024);
  }
  const unsigned hso = (unsigned)(((sbase + 32 + jj) * 128 + T0) * 1024 + wn * 512 + lane * 8);
  const unsigned hm = (unsigned)(((sbase + 48 + (jj >> 1)) * 128 + T0 + (lane >> 5)) * 1024 + (jj & 1) * 512 + ((2 * wn + (g & 1)) * 16 + tok) * 8);
  const int wm2 = r & 1, wn2 = r >> 1;
  const int hrd = wm2 * 4096 + lane * 16;
  unsigned drd[4];
  {
    const int f = q | ((g & 1) << 2);
#pragma unroll
    for (int u = 0; u < 4; ++u) drd[u] = lbase + H * G2_STAGE + 8192 + (8 * g + q) * 256 + (((4 * wn2 + u) ^ f) * 32) + 8 * p;
  }
  f32x4 acc[4][4];
#pragma unroll
  for (int t = 0; t < 4; ++t)
#pragma unroll
    for (int u = 0; u < 4; ++u) acc[t][u] = (f32x4){0.f, 0.f, 0.f, 0.f};
  const unsigned hra = lbase + H * G2_STAGE + hrd;
  const unsigned hra2 = lbase + H * G2_STAGE + wm2 * 4096 + lane * 8;
  WAIT_V(8);
  asm volatile("" ::"v"(pf3));
  BAR();
  if (wid == 0 && lane < 16) __hip_atomic_store(fin, 0ull, __ATOMIC_RELAXED, __HIP_MEMORY_SCOPE_AGENT);
  STAMP_NOW();
  bf16x4 dla[4], dha[4], dlb[4], dhb[4];
  bf16x8 ha[4], hb2[4];
  {
#pragma unroll
    for (int u = 0; u < 4; ++u) { dla[u] = tr_read<0>(drd[u]); dha[u] = tr_read<1024>(drd[u]); }
    ha[0] = ld128<0>(hra); ha[1] = ld128<1024>(hra); ha[2] = ld128<2048>(hra); ha[3] = ld128<3072>(hra);
  }
  int rs = 1, ws = 0;
  auto step2 = [&](int i, auto IL_, auto VM_, auto RD_, bf16x8(&hc)[4], bf16x8(&hn)[4], bf16x4(&dl)[4], bf16x4(&dh)[4], bf16x4(&dln)[4],
                  bf16x4(&dhn)[4]) __attribute__((always_inline)) {
    constexpr int IL = decltype(IL_)::value, VM = decltype(VM_)::value, RD = decltype(RD_)::value;
    const unsigned so = rs * (2 * G2_STAGE);
    const int S = 2 * (i + 5) + H;
    const int t0 = wd_t0(S), t1 = t0 + wd_dt(S);
    lds_u8* l = lds + ws * (2 * G2_STAGE) + H * G2_STAGE;
    constexpr int LL = IL;
    bf16x8 df;
    WAIT_L(0);
    if (RD) {
      WAIT_V(VM);
      BAR();
    }
    SCHED();
    df = cat8(dl[0], dh[0]);
    MF(acc[0][0], df, hc[0]);
    if (RD) dln[0] = tr_read<0>(drd[0] + so);
    SCHED();
    MF(acc[1][0], df, hc[1]);
    if (RD) dhn[0] = tr_read<1024>(drd[0] + so);
    SCHED();
    MF(acc[2][0], df, hc[2]);
    if (RD == 1) hn[0] = ld128<0>(hra + so);
    if (RD == 2) hn[0] = ld2x64<0>(hra2 + so);
    SCHED();
    MF(acc[3][0], df, hc[3]);
    if (RD == 1) hn[1] = ld128<1024>(hra + so);
    if (RD == 2) hn[1] = ld2x64<2>(hra2 + so);
    SCHED();
    df = cat8(dl[1], dh[1]);
    MF(acc[0][1], df, hc[0]);
    if (RD) dln[1] = tr_read<0>(drd[1] + so);
    SCHED();
    MF(acc[1][1], df, hc[1]);
    if (RD) dhn[1] = tr_read<1024>(drd[1] + so);
    SCHED();
    MF(acc[2][1], df, hc[2]);
    if (RD == 1) hn[2] = ld128<2048>(hra + so);
    if (RD == 2) hn[2] = ld2x64<4>(hra2 + so);
    SCHED();
    MF(acc[3][1], df, hc[3]);
    if (RD == 1) hn[3] = ld128<3072>(hra + so);
    if (RD == 2) hn[3] = ld2x64<6>(hra2 + so);
    SCHED();
    df = cat8(dl[2], dh[2]);
    MF(acc[0][2], df, hc[0]);
    if (RD) dln[2] = tr_read<0>(drd[2] + so);
    SCHED();
    MF(acc[1][2], df, hc[1]);
    if (RD) dhn[2] = tr_read<1024>(drd[2] + so);
    SCHED();
    MF(acc[2][2], df, hc[2]);
    if (RD) dln[3] = tr_read<0>(drd[3] + so);
    SCHED();
    MF(acc[3][2], df, hc[3]);
    if (RD) dhn[3] = tr_read<1024>(drd[3] + so);
    SCHED();
    df = cat8(dl[3], dh[3]);
    MF(acc[0][3], df, hc[0]);
    if (LL) glds16c(hb + (size_t)S * (128 * 1024) + voffH, l + r * 2048);
    SCHED();
    MF(acc[1][3], df, hc[1]);
    if (LL) glds16c(hb + (size_t)S * (128 * 1024) + 1024 + voffH, l + r * 2048 + 1024);
    SCHED();
    MF(acc[2][3], df, hc[2]);
    if (LL) glds16(db + (size_t)t0 * (16 * D_MODEL * 2) + voffD, l + 8192 + r * 2048);
    SCHED();
    MF(acc[3][3], df, hc[3]);
    if (LL) glds16(db + (size_t)t1 * (16 * D_MODEL * 2) + voffD, l + 8192 + r * 2048 + 1024);
    SCHED();
    rs = (rs == 4) ? 0 : rs + 1;
    ws = (ws == 4) ? 0 : ws + 1;
  };
#define G2_STEP_A(i, a, b, c) step2(i, IC<a>{}, IC<b>{}, IC<c>{}, ha, hb2, dla, dha, dlb, dhb)
#define G2_STEP_B(i, a, b, c) step2(i, IC<a>{}, IC<b>{}, IC<c>{}, hb2, ha, dlb, dhb, dla, dha)
  unsigned long long pv = 0;
  auto poll_issue = [&](gu64* f) __attribute__((always_inline)) {
    asm volatile("global_load_dwordx2 %0, %1, off sc1" : "+v"(pv) : "v"(f) : "memory");
  };
  auto poll_check = [&](gu64* f) __attribute__((always_inline)) {
    asm volatile("" : "+v"(pv));
    if (!__all(pv == MAGIC)) wait_flags(f);
    if (lane < 16) __hip_atomic_store(f, 0ull, __ATOMIC_RELAXED, __HIP_MEMORY_SCOPE_AGENT);
    __builtin_amdgcn_s_waitcnt(0xC07F);
  };
  G2_STEP_A(0, 1, 6, 1);
  __builtin_amdgcn_raw_buffer_store_b64(hs[0], rs_h, hso, 0, 16);
  __builtin_amdgcn_raw_buffer_store_b64(hs[1], rs_h, hso + 1024, 0, 16);
  G2_STEP_B(1, 1, 10, 1);
  __builtin_amdgcn_raw_buffer_store_b64(hs[2], rs_h, hso + 2048, 0, 16);
  __builtin_amdgcn_raw_buffer_store_b64(hs[3], rs_h, hso + 3072, 0, 16);
  G2_STEP_A(2, 1, 14, 1);
  __builtin_amdgcn_raw_buffer_store_b64(hmx[0], rs_h, hm, 0, 16);
  __builtin_amdgcn_raw_buffer_store_b64(hmx[1], rs_h, hm + 2048, 0, 16);
  STAMP_NOW();
  G2_STEP_B(3, 1, 18, 1);
  G2_STEP_A(4, 1, 18, 1);
  G2_STEP_B(5, 1, 16, 1);
  G2_STEP_A(6, 1, 14, 1);
  G2_STEP_B(7, 1, 12, 1);
  if (wid == 0 && lane < 16) __hip_atomic_store(fout + 16, MAGIC, __ATOMIC_RELAXED, __HIP_MEMORY_SCOPE_AGENT);
  G2_STEP_A(8, 1, 12, 1);
  G2_STEP_B(9, 1, 12, 1);
  if (wid == 0) poll_issue(fin + 16);
  G2_STEP_A(10, 1, 12, 1);
  STAMP_NOW();
  if (wid == 0) {
    WAIT_V(4);
    poll_check(fin + 16);
  }
  asm volatile("" ::: "memory");
  STAMP_NOW();
  G2_STEP_B(11, 1, 12, 1);
  G2_STEP_A(12, 1, 12, 1);
  G2_STEP_B(13, 1, 12, 1);
  G2_STEP_A(14, 1, 12, 1);
  G2_STEP_B(15, 1, 12, 2);
  for (int i = 16; i < 22; i += 2) {
    G2_STEP_A(i, 1, 12, 2);
    G2_STEP_B(i + 1, 1, 12, 2);
  }
  G2_STEP_A(22, 1, 12, 2);
  G2_STEP_B(23, 0, 12, 2);
  G2_STEP_A(24, 0, 8, 2);
  G2_STEP_B(25, 0, 4, 2);
  G2_STEP_A(26, 0, 0, 2);
  G2_STEP_B(27, 0, 0, 0);
  BAR();
  STAMP_NOW();

  f32x4 k0[4], k1[4];
  {
    lds_u8* dst = lds + ((1 - H) * 4 + r) * 8192 + lane * 16;
    if (H) {
#pragma unroll
      for (int u = 0; u < 4; ++u) {
        *(LAS f32x4*)(dst + u * 1024) = acc[0][u];
        *(LAS f32x4*)(dst + (4 + u) * 1024) = acc[1][u];
        k0[u] = acc[2][u]; k1[u] = acc[3][u];
      }
    } else {
#pragma unroll
      for (int u = 0; u < 4; ++u) {
        *(LAS f32x4*)(dst + u * 1024) = acc[2][u];
        *(LAS f32x4*)(dst + (4 + u) * 1024) = acc[3][u];
        k0[u] = acc[0][u]; k1[u] = acc[1][u];
      }
    }
  }
  __syncthreads();
  f32x4 keep[4], send[4];
  {
    lds_u8* src = lds + wid * 8192 + lane * 16;
#pragma unroll
    for (int u = 0; u < 4; ++u) {
      k0[u] += *(LAS f32x4*)(src + u * 1024);
      k1[u] += *(LAS f32x4*)(src + (4 + u) * 1024);
    }
    if (kh) {
#pragma unroll
      for (int u = 0; u < 4; ++u) { keep[u] = k1[u]; send[u] = k0[u]; }
    } else {
#pragma unroll
      for (int u = 0; u < 4; ++u) { keep[u] = k0[u]; send[u] = k1[u]; }
    }
  }
  STAMP_NOW();
  {
    const int pb = bid ^ 2;
    auto rs_x = __builtin_amdgcn_make_buffer_rsrc(hbuf + WS_XCH, 0, 256 * 32768, 0x00020000);
    const unsigned so = (unsigned)(pb * 32768 + wid * 4096 + lane * 16);
#pragma unroll
    for (int u = 0; u < 4; ++u) __builtin_amdgcn_raw_buffer_store_b128(__builtin_bit_cast(u32x4, send[u]), rs_x, so + u * 1024, 0, 16);
    WAIT_V(0);
    BAR();
    STAMP_NOW();
    if (wid == 0) {
      gu64* fl_out = (gu64*)(hbuf + WS_FLAG + (size_t)pb * 512 + 384);
      gu64* fl_in = (gu64*)(hbuf + WS_FLAG + (size_t)bid * 512 + 384);
      if (lane == 0) __hip_atomic_store(fl_out, MAGIC, __ATOMIC_RELAXED, __HIP_MEMORY_SCOPE_AGENT);
      const unsigned long long t0 = __builtin_amdgcn_s_memrealtime();
      for (;;) {
        const bool ok = __hip_atomic_load(fl_in, __ATOMIC_RELAXED, __HIP_MEMORY_SCOPE_AGENT) == MAGIC;
        if (__all(ok)) break;
        if (__builtin_amdgcn_s_memrealtime() - t0 > 5000000ull) break;
        __builtin_amdgcn_s_sleep(1);
      }
      if (lane == 0) __hip_atomic_store(fl_in, 0ull, __ATOMIC_RELAXED, __HIP_MEMORY_SCOPE_AGENT);
    }
    asm volatile("" ::: "memory");
    BAR();
    asm volatile("" ::: "memory");
    const unsigned si = (unsigned)(bid * 32768 + wid * 4096 + lane * 16);
#pragma unroll
    for (int u = 0; u < 4; ++u) keep[u] += __builtin_bit_cast(f32x4, __builtin_amdgcn_raw_buffer_load_b128(rs_x, si + u * 1024, 0, 16));
  }
  unsigned char* op = out + ((size_t)(mt2 * 128 + wm2 * 64 + (2 * H + kh) * 16 + tok) * D_MODEL + nt2 * 128 + wn2 * 64) * 2;
#pragma unroll
  for (int u = 0; u < 4; u += 2) {
    unsigned a0 = cvt_pk_bf16(keep[u][0], keep[u][1]), a1 = cvt_pk_bf16(keep[u][2], keep[u][3]);
    unsigned b0 = cvt_pk_bf16(keep[u + 1][0], keep[u + 1][1]), b1 = cvt_pk_bf16(keep[u + 1][2], keep[u + 1][3]);
    auto s0 = __builtin_amdgcn_permlane16_swap(a0, b0, false, false);
    auto s1 = __builtin_amdgcn_permlane16_swap(a1, b1, false, false);
    u32x4 v = {s0[0], s1[0], s0[1], s1[1]};
    const int col = (g & 1) ? (16 * (u + 1) + 4 * (g - 1)) : (16 * u + 4 * g);
    *(u32x4*)(op + col * 2) = v;
  }
  STAMP_NOW();
  STAMP_NOW();
  STAMP_OUT(hbuf + (64u << 20));
}

extern "C" void kernel_launch(void* const* d_in, const int* in_sizes, int n_in, void* d_out, int out_size, void* d_ws,
                              size_t ws_size, hipStream_t stream) {
  const unsigned char* x = (const unsigned char*)d_in[0];
  const unsigned char* wg = (const unsigned char*)d_in[1];
  const unsigned char* wu = (const unsigned char*)d_in[2];
  const unsigned char* wd = (const unsigned char*)d_in[3];
  unsigned char* hbuf = (unsigned char*)d_ws;
  (void)hipFuncSetAttribute((const void*)k_fused, hipFuncAttributeMaxDynamicSharedMemorySize, G1_LDS);
  k_fused<<<256, 512, G1_LDS, stream>>>(x, wg, wu, hbuf, wd, (unsigned char*)d_out);
}
```

```cpp
#include <hip/hip_runtime.h>
#include <hip/hip_bf16.h>
#include <stdint.h>
#define ROT ((xcd >> 2) * 8)

typedef short bf16x8 __attribute__((ext_vector_type(8)));
typedef short bf16x4 __attribute__((ext_vector_type(4)));
typedef float f32x4 __attribute__((ext_vector_type(4)));
typedef float f32x2 __attribute__((ext_vector_type(2)));
typedef unsigned u32x2 __attribute__((ext_vector_type(2)));
typedef unsigned u32x4 __attribute__((ext_vector_type(4)));
#define LAS __attribute__((address_space(3)))
typedef LAS unsigned char lds_u8;

constexpr int M_TOK = 2048, D_MODEL = 1024, F_FF = 3584;
constexpr size_t WS_XCH = 16u << 20;
constexpr size_t WS_FLAG = 32u << 20;

#define WAIT_V(n) asm volatile("s_waitcnt vmcnt(%0)" ::"n"(n) : "memory")
#define WAIT_L0() asm volatile("s_waitcnt lgkmcnt(0)" ::: "memory")
#define BAR() __builtin_amdgcn_s_barrier()
#define SCHED() __builtin_amdgcn_sched_barrier(0)
#define FINL __device__ __forceinline__

#define STAMP_DECL
#define STAMP_NOW()
#define STAMP_OUT(base)
template <int N> struct IC { static constexpr int value = N; };

FINL void glds16c(const void* g, lds_u8* l) {
  __builtin_amdgcn_global_load_lds((const unsigned*)g, (LAS unsigned*)l, 16, 0, 16);
}
FINL void glds16(const void* g, lds_u8* l) {
  __builtin_amdgcn_global_load_lds((const unsigned*)g, (LAS unsigned*)l, 16, 0, 0);
}
template <int OFF> FINL bf16x4 tr_read(unsigned addr) {
  bf16x4 r;
  asm volatile("ds_read_b64_tr_b16 %0, %1 offset:%2" : "=v"(r) : "v"(addr), "n"(OFF) : "memory");
  return r;
}
template <int OFF> FINL bf16x8 ld128(unsigned addr) {
  bf16x8 r;
  asm volatile("ds_read_b128 %0, %1 offset:%2" : "=v"(r) : "v"(addr), "n"(OFF) : "memory");
  return r;
}
template <int O0> FINL bf16x8 ld2x64(unsigned addr) {
  bf16x8 r;
  asm volatile("ds_read2st64_b64 %0, %1 offset0:%2 offset1:%3" : "=v"(r) : "v"(addr), "n"(O0), "n"(O0 + 1) : "memory");
  return r;
}
#define WAIT_L(n) asm volatile("s_waitcnt lgkmcnt(%0)" ::"n"(n) : "memory")
FINL bf16x8 cat8(bf16x4 a, bf16x4 b) { return __builtin_shufflevector(a, b, 0, 1, 2, 3, 4, 5, 6, 7); }
FINL unsigned cvt_pk_bf16(float lo, float hi) {
  unsigned r;
  asm("v_cvt_pk_bf16_f32 %0, %1, %2" : "=v"(r) : "v"(lo), "v"(hi));
  return r;
}
FINL f32x2 geglu2(f32x2 g, f32x2 u) {
  const float c0 = -2.0f * 0.7978845608028654f * 1.4426950408889634f;
  const float c1 = c0 * 0.044715f;
  f32x2 t = g * g;
  f32x2 p = t * c1 + c0;
  f32x2 a = g * p;
  f32x2 e;
  e.x = __builtin_amdgcn_exp2f(a.x);
  e.y = __builtin_amdgcn_exp2f(a.y);
  f32x2 d = e + 1.0f;
  f32x2 r;
  r.x = __builtin_amdgcn_rcpf(d.x);
  r.y = __builtin_amdgcn_rcpf(d.y);
  return (g * r) * u;
}
FINL u32x2 geglu4(f32x4 g, f32x4 u) {
  f32x2 a = geglu2((f32x2){g[0], g[1]}, (f32x2){u[0], u[1]});
  f32x2 b = geglu2((f32x2){g[2], g[3]}, (f32x2){u[2], u[3]});
  u32x2 o;
  o.x = cvt_pk_bf16(a.x, a.y);
  o.y = cvt_pk_bf16(b.x, b.y);
  return o;
}

constexpr int G1_WGRP = 2048;
constexpr int G1_WSLOT = 8 * G1_WGRP;
constexpr int G1_NW = 4;
constexpr int G1_XSLOT = 32768;
constexpr int G1_NX = 3;
constexpr int G1_XOFF = G1_NW * G1_WSLOT;
constexpr int G1_LDS = G1_XOFF + G1_NX * G1_XSLOT;

constexpr int G2_STAGE = 16384;
constexpr unsigned long long MAGIC = 0x5E1F0A3C97B2D461ull;
typedef __attribute__((address_space(1))) unsigned long long gu64;

__global__ __launch_bounds__(512, 2) void k_fused(const unsigned char* __restrict__ x, const unsigned char* __restrict__ wg,
                                                  const unsigned char* __restrict__ wu, unsigned char* hbuf,
                                                  const unsigned char* __restrict__ wd, unsigned char* __restrict__ out) {
  extern __shared__ __attribute__((aligned(16))) unsigned char smem_raw[];
  lds_u8* lds = (lds_u8*)smem_raw;
  const int tid = threadIdx.x;
  const int wid = __builtin_amdgcn_readfirstlane(tid >> 6);
  const int lane = tid & 63;
  const int half = wid >> 2, wm = wid & 3, wn = wid >> 2;
  STAMP_DECL
  STAMP_NOW();
  const int bid = blockIdx.x;
  const int xcd = bid & 7, idx = bid >> 3;
  const int mt = (xcd >> 2) * 4 + (idx & 3);
  const int nt = (xcd & 3) * 8 + (idx >> 2);
  const int g = lane >> 4, q = (lane >> 2) & 3, p = lane & 3, tok = lane & 15;

  int li = lane - ((wid >> 1) & 1) * 8;
  li = (li < 0 || li >= 56) ? 0 : li;
  const unsigned voffW = (unsigned)((4 * wid + li / 14) * (F_FF * 2) + (li % 14) * 16);
  unsigned voffX[2];
#pragma unroll
  for (int i = 0; i < 2; ++i)
    voffX[i] = (unsigned)((8 * (2 * wid + i) + (lane >> 3)) * (D_MODEL * 2) + (((lane & 7) ^ ((i << 2) | (lane >> 4))) * 16));
  const unsigned char* wgb = wg + (size_t)nt * 224;
  const unsigned char* wub = wu + (size_t)nt * 224;
  const unsigned char* xb = x + (size_t)mt * 256 * (D_MODEL * 2);
  const int wdst = wid * G1_WGRP;
  const int xdst = G1_XOFF + wid * 2048;

  const int rot2 = (ROT) & 15;
  auto stageW = [&](int j, int slot) __attribute__((always_inline)) {
    const size_t go = (size_t)((j + 2 * rot2) & 31) * (32 * F_FF * 2);
    glds16(wgb + go + voffW, lds + slot * G1_WSLOT + wdst);
    glds16(wub + go + voffW, lds + slot * G1_WSLOT + wdst + 1024);
  };
  auto stageX = [&](int t, int hb, int slot) __attribute__((always_inline)) {
    const size_t go = (size_t)hb * (128 * D_MODEL * 2) + (size_t)((t + rot2) & 15) * 128;
    glds16(xb + go + voffX[0], lds + xdst + slot * G1_XSLOT + hb * 16384);
    glds16(xb + go + voffX[1], lds + xdst + slot * G1_XSLOT + hb * 16384 + 1024);
  };

  const unsigned lbase = (unsigned)(size_t)lds;
  const unsigned wrd = lbase + 2 * g * G1_WGRP + (g & 1) * 128 + q * 224 + p * 8 + wn * 128;
  const unsigned wrdm = lbase + 2 * g * G1_WGRP + (g & 1) * 128 + q * 224 + 96 + wn * 16 + (p & 1) * 8 + (p >> 1) * 1024;
  int xrd[2];
#pragma unroll
  for (int s = 0; s < 2; ++s) xrd[s] = G1_XOFF + (wm * 64 + tok) * 128 + ((((s << 2) | g) ^ ((tok >> 1) & 7)) * 16);

  f32x4 accg[4][3], accu[4][3], accm[4];
#pragma unroll
  for (int t = 0; t < 4; ++t) {
#pragma unroll
    for (int u = 0; u < 3; ++u) { accg[t][u] = (f32x4){0.f, 0.f, 0.f, 0.f}; accu[t][u] = (f32x4){0.f, 0.f, 0.f, 0.f}; }
    accm[t] = (f32x4){0.f, 0.f, 0.f, 0.f};
  }

  const int H = wid >> 2, r = wid & 3;
  const int kh = (xcd >> 1) & 1;
  const int mt2 = (xcd >> 2) * 8 + (idx & 7);
  const int nt2 = (xcd & 1) * 4 + (idx >> 3);
  const unsigned char* hb = hbuf + ((size_t)kh * 56 * 128 + mt2 * 8 + 2 * r) * 1024;
  const unsigned char* db = wd + (size_t)kh * 56 * 32 * (D_MODEL * 2) + (size_t)nt2 * 256;
  stageX(0, 0, 0); stageX(0, 1, 0); stageW(0, 0);
  stageW(1, 1); stageX(1, 0, 1);
  stageW(2, 2); stageX(1, 1, 1);
  stageW(3, 3); stageX(2, 0, 2);
  WAIT_V(12);
  BAR();
  STAMP_NOW();

  const unsigned xra[2] = {lbase + (unsigned)xrd[0], lbase + (unsigned)xrd[1]};
  bf16x4 wl[7], wh[7];
  bf16x8 xa[4], xb2[4];
  {
    wl[0] = tr_read<0>(wrd); wh[0] = tr_read<G1_WGRP>(wrd);
    wl[1] = tr_read<32>(wrd); wh[1] = tr_read<32 + G1_WGRP>(wrd);
    wl[2] = tr_read<64>(wrd); wh[2] = tr_read<64 + G1_WGRP>(wrd);
    wl[3] = tr_read<1024>(wrd); wh[3] = tr_read<1024 + G1_WGRP>(wrd);
    wl[4] = tr_read<1024 + 32>(wrd); wh[4] = tr_read<1024 + 32 + G1_WGRP>(wrd);
    wl[5] = tr_read<1024 + 64>(wrd); wh[5] = tr_read<1024 + 64 + G1_WGRP>(wrd);
    wl[6] = tr_read<0>(wrdm); wh[6] = tr_read<G1_WGRP>(wrdm);
    xa[0] = ld128<0>(xra[0]); xa[1] = ld128<2048>(xra[0]); xa[2] = ld128<4096>(xra[0]); xa[3] = ld128<6144>(xra[0]);
    WAIT_L(0);
  }
  int xs_rd = 0, xs_wr = 2;
  auto step = [&](int k, auto S_, auto IW_, auto IX_, auto VM_, auto RD_, bf16x8(&xc)[4], bf16x8(&xn)[4], auto IP_) __attribute__((always_inline)) {
    constexpr int S = decltype(S_)::value;
    constexpr int IP = decltype(IP_)::value;
    constexpr int IW = decltype(IW_)::value, IX = decltype(IX_)::value, VM = decltype(VM_)::value, RD = decltype(RD_)::value;
    if (S) xs_rd = (xs_rd == 2) ? 0 : xs_rd + 1;
    const unsigned wb = ((k + 1) & 3) * G1_WSLOT + wrd;
    const unsigned wbm = ((k + 1) & 3) * G1_WSLOT + wrdm;
    const unsigned xbp = xs_rd * G1_XSLOT + xra[S ^ 1];
#define MF(ACC, WF, XF) ACC = __builtin_amdgcn_mfma_f32_16x16x32_bf16(WF, XF, ACC, 0, 0, 0); SCHED();
    constexpr int LW = IW, LX = IX;
    const size_t goW = (size_t)((k + 4 + 2 * rot2) & 31) * (32 * F_FF * 2);
    const size_t goX = (size_t)(S ^ 1) * (128 * D_MODEL * 2) + (size_t)((((k + 1) >> 1) + 2 + rot2) & 15) * 128;
    lds_u8* ldW = lds + (k & 3) * G1_WSLOT + wdst;
    lds_u8* ldX = lds + xdst + xs_wr * G1_XSLOT + (S ^ 1) * 16384;
    bf16x8 wf;
    WAIT_L(10);
    SCHED();
    wf = cat8(wl[0], wh[0]);
    MF(accg[0][0], wf, xc[0]);
    MF(accg[1][0], wf, xc[1]);
    MF(accg[2][0], wf, xc[2]);
    MF(accg[3][0], wf, xc[3]);
    WAIT_L(0);
    if (RD) {
      if (VM >= 0) WAIT_V(VM);
      BAR();
    }
    SCHED();
    wf = cat8(wl[3], wh[3]);
    MF(accu[0][0], wf, xc[0]);
    if (RD) wl[0] = tr_read<0>(wb);
    SCHED();
    MF(accu[1][0], wf, xc[1]);
    if (RD) wh[0] = tr_read<0 + G1_WGRP>(wb);
    SCHED();
    MF(accu[2][0], wf, xc[2]);
    if (RD) xn[0] = ld128<0>(xbp);
    SCHED();
    MF(accu[3][0], wf, xc[3]);
    if (RD) xn[1] = ld128<2048>(xbp);
    SCHED();
    wf = cat8(wl[1], wh[1]);
    MF(accg[0][1], wf, xc[0]);
    if (RD) wl[3] = tr_read<1024>(wb);
    SCHED();
    MF(accg[1][1], wf, xc[1]);
    if (RD) wh[3] = tr_read<1024 + G1_WGRP>(wb);
    SCHED();
    MF(accg[2][1], wf, xc[2]);
    if (RD) xn[2] = ld128<4096>(xbp);
    SCHED();
    MF(accg[3][1], wf, xc[3]);
    if (RD) xn[3] = ld128<6144>(xbp);
    SCHED();
    wf = cat8(wl[4], wh[4]);
    MF(accu[0][1], wf, xc[0]);
    if (RD) wl[1] = tr_read<32>(wb);
    SCHED();
    MF(accu[1][1], wf, xc[1]);
    if (RD) wh[1] = tr_read<32 + G1_WGRP>(wb);
    SCHED();
    MF(accu[2][1], wf, xc[2]);
    if (LW) glds16(wgb + goW + voffW, ldW);
    SCHED();
    MF(accu[3][1], wf, xc[3]);
    wf = cat8(wl[2], wh[2]);
    MF(accg[0][2], wf, xc[0]);
    if (RD) wl[4] = tr_read<1024 + 32>(wb);
    SCHED();
    MF(accg[1][2], wf, xc[1]);
    if (RD) wh[4] = tr_read<1024 + 32 + G1_WGRP>(wb);
    SCHED();
    MF(accg[2][2], wf, xc[2]);
    if (LW) glds16(wub + goW + voffW, ldW + 1024);
    SCHED();
    MF(accg[3][2], wf, xc[3]);
    wf = cat8(wl[5], wh[5]);
    MF(accu[0][2], wf, xc[0]);
    if (RD) wl[2] = tr_read<64>(wb);
    SCHED();
    MF(accu[1][2], wf, xc[1]);
    if (RD) wh[2] = tr_read<64 + G1_WGRP>(wb);
    SCHED();
    MF(accu[2][2], wf, xc[2]);
    if (LX) glds16(xb + goX + voffX[0], ldX);
    SCHED();
    MF(accu[3][2], wf, xc[3]);
    wf = cat8(wl[6], wh[6]);
    MF(accm[0], wf, xc[0]);
    if (RD) wl[5] = tr_read<1024 + 64>(wb);
    SCHED();
    MF(accm[1], wf, xc[1]);
    if (RD) wh[5] = tr_read<1024 + 64 + G1_WGRP>(wb);
    SCHED();
    MF(accm[2], wf, xc[2]);
    if (LX) glds16(xb + goX + voffX[1], ldX + 1024);
    SCHED();
    MF(accm[3], wf, xc[3]);
    if (RD) wl[6] = tr_read<0>(wbm);
    if (RD) wh[6] = tr_read<0 + G1_WGRP>(wbm);
    SCHED();
    if (!S) xs_wr = (xs_wr == 2) ? 0 : xs_wr + 1;
  };
  for (int k = 0; k < 26; k += 2) {
    step(k, IC<0>{}, IC<1>{}, IC<1>{}, IC<8>{}, IC<1>{}, xa, xb2, IC<0>{});
    step(k + 1, IC<1>{}, IC<1>{}, IC<1>{}, IC<8>{}, IC<1>{}, xb2, xa, IC<0>{});
  }
  step(26, IC<0>{}, IC<1>{}, IC<1>{}, IC<8>{}, IC<1>{}, xa, xb2, IC<0>{});
  step(27, IC<1>{}, IC<1>{}, IC<0>{}, IC<8>{}, IC<1>{}, xb2, xa, IC<0>{});
  {
    bf16x8 wf;
    WAIT_L(0);
    WAIT_V(6);
    BAR();
    SCHED();
    wf = cat8(wl[0], wh[0]);
    MF(accg[0][0], wf, xa[0]);
    xb2[0] = ld128<0>((xra[1] + 2 * G1_XSLOT));
    SCHED();
    MF(accg[1][0], wf, xa[1]);
    xb2[1] = ld128<2048>((xra[1] + 2 * G1_XSLOT));
    SCHED();
    MF(accg[2][0], wf, xa[2]);
    xb2[2] = ld128<4096>((xra[1] + 2 * G1_XSLOT));
    SCHED();
    MF(accg[3][0], wf, xa[3]);
    xb2[3] = ld128<6144>((xra[1] + 2 * G1_XSLOT));
    SCHED();
    wf = cat8(wl[3], wh[3]);
    MF(accu[0][0], wf, xa[0]);
    wl[0] = tr_read<0>((wrd + 1 * G1_WSLOT));
    SCHED();
    MF(accu[1][0], wf, xa[1]);
    wh[0] = tr_read<0 + G1_WGRP>((wrd + 1 * G1_WSLOT));
    SCHED();
    MF(accu[2][0], wf, xa[2]);
    MF(accu[3][0], wf, xa[3]);
    wf = cat8(wl[1], wh[1]);
    MF(accg[0][1], wf, xa[0]);
    wl[3] = tr_read<1024>((wrd + 1 * G1_WSLOT));
    SCHED();
    MF(accg[1][1], wf, xa[1]);
    wh[3] = tr_read<1024 + G1_WGRP>((wrd + 1 * G1_WSLOT));
    SCHED();
    MF(accg[2][1], wf, xa[2]);
    MF(accg[3][1], wf, xa[3]);
    wf = cat8(wl[4], wh[4]);
    MF(accu[0][1], wf, xa[0]);
    wl[1] = tr_read<32>((wrd + 1 * G1_WSLOT));
    SCHED();
    MF(accu[1][1], wf, xa[1]);
    wh[1] = tr_read<32 + G1_WGRP>((wrd + 1 * G1_WSLOT));
    SCHED();
    MF(accu[2][1], wf, xa[2]);
    MF(accu[3][1], wf, xa[3]);
    wl[4] = tr_read<1024 + 32>((wrd + 1 * G1_WSLOT));
    wh[4] = tr_read<1024 + 32 + G1_WGRP>((wrd + 1 * G1_WSLOT));
    SCHED();
  }
  {
    bf16x8 wf;
    WAIT_L(0);
    WAIT_V(2);
    BAR();
    SCHED();
    wf = cat8(wl[0], wh[0]);
    MF(accg[0][0], wf, xb2[0]);
    xa[0] = ld128<0>((xra[0] + 0 * G1_XSLOT));
    SCHED();
    MF(accg[1][0], wf, xb2[1]);
    xa[1] = ld128<2048>((xra[0] + 0 * G1_XSLOT));
    SCHED();
    MF(accg[2][0], wf, xb2[2]);
    xa[2] = ld128<4096>((xra[0] + 0 * G1_XSLOT));
    SCHED();
    MF(accg[3][0], wf, xb2[3]);
    xa[3] = ld128<6144>((xra[0] + 0 * G1_XSLOT));
    SCHED();
    wf = cat8(wl[3], wh[3]);
    MF(accu[0][0], wf, xb2[0]);
    wl[0] = tr_read<0>((wrd + 2 * G1_WSLOT));
    SCHED();
    MF(accu[1][0], wf, xb2[1]);
    wh[0] = tr_read<0 + G1_WGRP>((wrd + 2 * G1_WSLOT));
    SCHED();
    MF(accu[2][0], wf, xb2[2]);
    MF(accu[3][0], wf, xb2[3]);
    wf = cat8(wl[1], wh[1]);
    MF(accg[0][1], wf, xb2[0]);
    wl[3] = tr_read<1024>((wrd + 2 * G1_WSLOT));
    SCHED();
    MF(accg[1][1], wf, xb2[1]);
    wh[3] = tr_read<1024 + G1_WGRP>((wrd + 2 * G1_WSLOT));
    SCHED();
    MF(accg[2][1], wf, xb2[2]);
    MF(accg[3][1], wf, xb2[3]);
    wf = cat8(wl[4], wh[4]);
    MF(accu[0][1], wf, xb2[0]);
    wl[1] = tr_read<32>((wrd + 2 * G1_WSLOT));
    SCHED();
    MF(accu[1][1], wf, xb2[1]);
    wh[1] = tr_read<32 + G1_WGRP>((wrd + 2 * G1_WSLOT));
    SCHED();
    MF(accu[2][1], wf, xb2[2]);
    MF(accu[3][1], wf, xb2[3]);
    wl[4] = tr_read<1024 + 32>((wrd + 2 * G1_WSLOT));
    wh[4] = tr_read<1024 + 32 + G1_WGRP>((wrd + 2 * G1_WSLOT));
    SCHED();
  }
  {
    bf16x8 wf;
    WAIT_L(0);
    WAIT_V(0);
    BAR();
    SCHED();
    wf = cat8(wl[0], wh[0]);
    MF(accg[0][0], wf, xa[0]);
    xb2[0] = ld128<0>((xra[1] + 0 * G1_XSLOT));
    SCHED();
    MF(accg[1][0], wf, xa[1]);
    xb2[1] = ld128<2048>((xra[1] + 0 * G1_XSLOT));
    SCHED();
    MF(accg[2][0], wf, xa[2]);
    xb2[2] = ld128<4096>((xra[1] + 0 * G1_XSLOT));
    SCHED();
    MF(accg[3][0], wf, xa[3]);
    xb2[3] = ld128<6144>((xra[1] + 0 * G1_XSLOT));
    SCHED();
    wf = cat8(wl[3], wh[3]);
    MF(accu[0][0], wf, xa[0]);
    wl[0] = tr_read<0>((wrd + 3 * G1_WSLOT));
    SCHED();
    MF(accu[1][0], wf, xa[1]);
    wh[0] = tr_read<0 + G1_WGRP>((wrd + 3 * G1_WSLOT));
    SCHED();
    MF(accu[2][0], wf, xa[2]);
    MF(accu[3][0], wf, xa[3]);
    wf = cat8(wl[1], wh[1]);
    MF(accg[0][1], wf, xa[0]);
    wl[3] = tr_read<1024>((wrd + 3 * G1_WSLOT));
    SCHED();
    MF(accg[1][1], wf, xa[1]);
    wh[3] = tr_read<1024 + G1_WGRP>((wrd + 3 * G1_WSLOT));
    SCHED();
    MF(accg[2][1], wf, xa[2]);
    MF(accg[3][1], wf, xa[3]);
    wf = cat8(wl[4], wh[4]);
    MF(accu[0][1], wf, xa[0]);
    wl[1] = tr_read<32>((wrd + 3 * G1_WSLOT));
    SCHED();
    MF(accu[1][1], wf, xa[1]);
    wh[1] = tr_read<32 + G1_WGRP>((wrd + 3 * G1_WSLOT));
    SCHED();
    MF(accu[2][1], wf, xa[2]);
    MF(accu[3][1], wf, xa[3]);
    wl[4] = tr_read<1024 + 32>((wrd + 3 * G1_WSLOT));
    wh[4] = tr_read<1024 + 32 + G1_WGRP>((wrd + 3 * G1_WSLOT));
    SCHED();
  }
  unsigned pf3;
  {
    const int e2 = (wid < 7 ? wid : 6) * 64 + lane;
    const unsigned char* pw = wd + (size_t)(kh * 1792 + idx * 56 + (e2 >> 3)) * 2048 + (xcd & 1) * 1024 + (e2 & 7) * 128;
    asm volatile("global_load_dword %0, %1, off" : "=v"(pf3) : "v"(pw) : "memory");
  }
  {
    bf16x8 wf;
    WAIT_L(0);
    SCHED();
    wf = cat8(wl[0], wh[0]);
    MF(accg[0][0], wf, xb2[0]);
    xa[0] = ld128<0>((xra[0] + 2 * G1_XSLOT));
    SCHED();
    MF(accg[1][0], wf, xb2[1]);
    xa[1] = ld128<2048>((xra[0] + 2 * G1_XSLOT));
    SCHED();
    MF(accg[2][0], wf, xb2[2]);
    xa[2] = ld128<4096>((xra[0] + 2 * G1_XSLOT));
    SCHED();
    MF(accg[3][0], wf, xb2[3]);
    xa[3] = ld128<6144>((xra[0] + 2 * G1_XSLOT));
    SCHED();
    wf = cat8(wl[3], wh[3]);
    MF(accu[0][0], wf, xb2[0]);
    MF(accu[1][0], wf, xb2[1]);
    MF(accu[2][0], wf, xb2[2]);
    MF(accu[3][0], wf, xb2[3]);
    wf = cat8(wl[1], wh[1]);
    MF(accg[0][1], wf, xb2[0]);
    MF(accg[1][1], wf, xb2[1]);
    MF(accg[2][1], wf, xb2[2]);
    MF(accg[3][1], wf, xb2[3]);
    wf = cat8(wl[4], wh[4]);
    MF(accu[0][1], wf, xb2[0]);
    MF(accu[1][1], wf, xb2[1]);
    MF(accu[2][1], wf, xb2[2]);
    MF(accu[3][1], wf, xb2[3]);
    SCHED();
  }
  STAMP_NOW();
  auto rs_h = __builtin_amdgcn_make_buffer_rsrc(hbuf, 0, 112 * 128 * 1024, 0x00020000);
  const int T0 = mt * 16 + wm * 4;
  const int sbase = (nt >> 4) * 56, jj = nt & 15;
  {
    const unsigned ho = (unsigned)(((sbase + 2 * jj + wn) * 128 + T0) * 1024 + lane * 16);
#pragma unroll
    for (int t = 0; t < 4; ++t) {
      const u32x2 a = geglu4(accg[t][0], accu[t][0]), b = geglu4(accg[t][1], accu[t][1]);
      __builtin_amdgcn_raw_buffer_store_b128((u32x4){a.x, a.y, b.x, b.y}, rs_h, ho + t * 1024, 0, 16);
    }
  }
  SCHED();
  STAMP_NOW();
  {
    bf16x8 wf;
    WAIT_L(0);
    SCHED();
    wf = cat8(wl[2], wh[2]);
    MF(accg[0][2], wf, xa[0]);
    xb2[0] = ld128<0>((xra[1] + 2 * G1_XSLOT));
    SCHED();
    MF(accg[1][2], wf, xa[1]);
    xb2[1] = ld128<2048>((xra[1] + 2 * G1_XSLOT));
    SCHED();
    MF(accg[2][2], wf, xa[2]);
    xb2[2] = ld128<4096>((xra[1] + 2 * G1_XSLOT));
    SCHED();
    MF(accg[3][2], wf, xa[3]);
    xb2[3] = ld128<6144>((xra[1] + 2 * G1_XSLOT));
    SCHED();
    wf = cat8(wl[5], wh[5]);
    MF(accu[0][2], wf, xa[0]);
    wl[2] = tr_read<64>((wrd + 1 * G1_WSLOT));
    SCHED();
    MF(accu[1][2], wf, xa[1]);
    wh[2] = tr_read<64 + G1_WGRP>((wrd + 1 * G1_WSLOT));
    SCHED();
    MF(accu[2][2], wf, xa[2]);
    MF(accu[3][2], wf, xa[3]);
    wf = cat8(wl[6], wh[6]);
    MF(accm[0], wf, xa[0]);
    wl[5] = tr_read<1024 + 64>((wrd + 1 * G1_WSLOT));
    SCHED();
    MF(accm[1], wf, xa[1]);
    wh[5] = tr_read<1024 + 64 + G1_WGRP>((wrd + 1 * G1_WSLOT));
    SCHED();
    MF(accm[2], wf, xa[2]);
    MF(accm[3], wf, xa[3]);
    wl[6] = tr_read<0>((wrdm + 1 * G1_WSLOT));
    wh[6] = tr_read<0 + G1_WGRP>((wrdm + 1 * G1_WSLOT));
    SCHED();
  }
  {
    bf16x8 wf;
    WAIT_L(0);
    SCHED();
    wf = cat8(wl[2], wh[2]);
    MF(accg[0][2], wf, xb2[0]);
    xa[0] = ld128<0>((xra[0] + 0 * G1_XSLOT));
    SCHED();
    MF(accg[1][2], wf, xb2[1]);
    xa[1] = ld128<2048>((xra[0] + 0 * G1_XSLOT));
    SCHED();
    MF(accg[2][2], wf, xb2[2]);
    xa[2] = ld128<4096>((xra[0] + 0 * G1_XSLOT));
    SCHED();
    MF(accg[3][2], wf, xb2[3]);
    xa[3] = ld128<6144>((xra[0] + 0 * G1_XSLOT));
    SCHED();
    wf = cat8(wl[5], wh[5]);
    MF(accu[0][2], wf, xb2[0]);
    wl[2] = tr_read<64>((wrd + 2 * G1_WSLOT));
    SCHED();
    MF(accu[1][2], wf, xb2[1]);
    wh[2] = tr_read<64 + G1_WGRP>((wrd + 2 * G1_WSLOT));
    SCHED();
    MF(accu[2][2], wf, xb2[2]);
    MF(accu[3][2], wf, xb2[3]);
    wf = cat8(wl[6], wh[6]);
    MF(accm[0], wf, xb2[0]);
    wl[5] = tr_read<1024 + 64>((wrd + 2 * G1_WSLOT));
    SCHED();
    MF(accm[1], wf, xb2[1]);
    wh[5] = tr_read<1024 + 64 + G1_WGRP>((wrd + 2 * G1_WSLOT));
    SCHED();
    MF(accm[2], wf, xb2[2]);
    MF(accm[3], wf, xb2[3]);
    wl[6] = tr_read<0>((wrdm + 2 * G1_WSLOT));
    wh[6] = tr_read<0 + G1_WGRP>((wrdm + 2 * G1_WSLOT));
    SCHED();
  }
  {
    bf16x8 wf;
    WAIT_L(0);
    SCHED();
    wf = cat8(wl[2], wh[2]);
    MF(accg[0][2], wf, xa[0]);
    xb2[0] = ld128<0>((xra[1] + 0 * G1_XSLOT));
    SCHED();
    MF(accg[1][2], wf, xa[1]);
    xb2[1] = ld128<2048>((xra[1] + 0 * G1_XSLOT));
    SCHED();
    MF(accg[2][2], wf, xa[2]);
    xb2[2] = ld128<4096>((xra[1] + 0 * G1_XSLOT));
    SCHED();
    MF(accg[3][2], wf, xa[3]);
    xb2[3] = ld128<6144>((xra[1] + 0 * G1_XSLOT));
    SCHED();
    wf = cat8(wl[5], wh[5]);
    MF(accu[0][2], wf, xa[0]);
    wl[2] = tr_read<64>((wrd + 3 * G1_WSLOT));
    SCHED();
    MF(accu[1][2], wf, xa[1]);
    wh[2] = tr_read<64 + G1_WGRP>((wrd + 3 * G1_WSLOT));
    SCHED();
    MF(accu[2][2], wf, xa[2]);
    MF(accu[3][2], wf, xa[3]);
    wf = cat8(wl[6], wh[6]);
    MF(accm[0], wf, xa[0]);
    wl[5] = tr_read<1024 + 64>((wrd + 3 * G1_WSLOT));
    SCHED();
    MF(accm[1], wf, xa[1]);
    wh[5] = tr_read<1024 + 64 + G1_WGRP>((wrd + 3 * G1_WSLOT));
    SCHED();
    MF(accm[2], wf, xa[2]);
    MF(accm[3], wf, xa[3]);
    wl[6] = tr_read<0>((wrdm + 3 * G1_WSLOT));
    wh[6] = tr_read<0 + G1_WGRP>((wrdm + 3 * G1_WSLOT));
    SCHED();
  }
  {
    bf16x8 wf;
    WAIT_L(0);
    SCHED();
    wf = cat8(wl[2], wh[2]);
    MF(accg[0][2], wf, xb2[0]);
    MF(accg[1][2], wf, xb2[1]);
    MF(accg[2][2], wf, xb2[2]);
    MF(accg[3][2], wf, xb2[3]);
    wf = cat8(wl[5], wh[5]);
    MF(accu[0][2], wf, xb2[0]);
    MF(accu[1][2], wf, xb2[1]);
    MF(accu[2][2], wf, xb2[2]);
    MF(accu[3][2], wf, xb2[3]);
    wf = cat8(wl[6], wh[6]);
    MF(accm[0], wf, xb2[0]);
    MF(accm[1], wf, xb2[1]);
    MF(accm[2], wf, xb2[2]);
    MF(accm[3], wf, xb2[3]);
    SCHED();
  }
  WAIT_V(0);
  BAR();
  STAMP_NOW();
  const unsigned voffH = (unsigned)(lane * 16);
  const unsigned voffD = (unsigned)((4 * r + g) * (D_MODEL * 2) + ((((tok >> 1) ^ (g | ((r & 1) << 2))) << 1) | (lane & 1)) * 16);
  gu64* fout;
  gu64* fin = (gu64*)(hbuf + WS_FLAG + (size_t)bid * 512 + (lane & 15) * 8);
  {
    const int cm = 2 * mt + (lane & 1), cn = (lane >> 1) & 7;
    const int cb = (((cm & 7) | ((cn & 3) << 3)) << 3) | ((cm >> 3) << 2) | ((nt >> 4) << 1) | (cn >> 2);
    fout = (gu64*)(hbuf + WS_FLAG + (size_t)cb * 512 + (nt & 15) * 8);
  }
  if (wid == 0 && lane < 16) __hip_atomic_store(fout, MAGIC, __ATOMIC_RELAXED, __HIP_MEMORY_SCOPE_AGENT);
  auto wd_t0 = [&](int sg) __attribute__((always_inline)) -> int {
    return sg < 32 ? 7 * (sg >> 1) + 4 * (sg & 1) : sg < 48 ? 7 * (sg - 32) + 2 : 14 * (sg - 48) + 3;
  };
  auto wd_dt = [&](int sg) __attribute__((always_inline)) -> int { return sg < 32 ? 1 : sg < 48 ? 4 : 7; };
#pragma unroll
  for (int i = 0; i < 5; ++i) {
    const int t0 = wd_t0(2 * i + H);
    lds_u8* l = lds + (2 * i + H) * G2_STAGE;
    glds16(db + (size_t)t0 * (16 * D_MODEL * 2) + voffD, l + 8192 + r * 2048);
    glds16(db + (size_t)(t0 + 1) * (16 * D_MODEL * 2) + voffD, l + 8192 + r * 2048 + 1024);
  }
  u32x2 hs[4], hmx[2];
#pragma unroll
  for (int t = 0; t < 4; ++t) hs[t] = geglu4(accg[t][2], accu[t][2]);
#pragma unroll
  for (int t = 0; t < 4; t += 2) {
    f32x4 gg, uu;
#pragma unroll
    for (int rr = 0; rr < 4; ++rr) {
      auto sw = __builtin_amdgcn_permlane32_swap(__float_as_uint(accm[t][rr]), __float_as_uint(accm[t + 1][rr]), false, false);
      gg[rr] = __uint_as_float(sw[0]);
      uu[rr] = __uint_as_float(sw[1]);
    }
    hmx[t >> 1] = geglu4(gg, uu);
  }
  asm volatile("" : "+v"(hs[0]), "+v"(hs[1]), "+v"(hs[2]), "+v"(hs[3]), "+v"(hmx[0]), "+v"(hmx[1]));
  STAMP_NOW();
  auto wait_flags = [&](gu64* f) __attribute__((always_inline)) {
    const unsigned long long t0 = __builtin_amdgcn_s_memrealtime();
    for (;;) {
      const bool ok = __hip_atomic_load(f, __ATOMIC_RELAXED, __HIP_MEMORY_SCOPE_AGENT) == MAGIC;
      if (__all(ok)) break;
      if (__builtin_amdgcn_s_memrealtime() - t0 > 5000000ull) break;
      __builtin_amdgcn_s_sleep(1);
    }
  };
  if (wid == 0) {
    wait_flags(fin);
    __builtin_amdgcn_s_waitcnt(0xC07F);
  }
  asm volatile("" ::: "memory");
  BAR();
  asm volatile("" ::: "memory");
  STAMP_NOW();

#pragma unroll
  for (int i = 0; i < 5; ++i) {
    const int S = 2 * i + H;
    lds_u8* l = lds + S * G2_STAGE;
    glds16c(hb + (size_t)S * (128 * 1024) + voffH, l + r * 2048);
    glds16c(hb + (size_t)S * (128 * 1024) + 1024 + voffH, l + r * 2048 + 1024);
  }
  const unsigned hso = (unsigned)(((sbase + 32 + jj) * 128 + T0) * 1024 + wn * 512 + lane * 8);
  const unsigned hm = (unsigned)(((sbase + 48 + (jj >> 1)) * 128 + T0 + (lane >> 5)) * 1024 + (jj & 1) * 512 + ((2 * wn + (g & 1)) * 16 + tok) * 8);
  const int wm2 = r & 1, wn2 = r >> 1;
  const int hrd = wm2 * 4096 + lane * 16;
  unsigned drd[4];
  {
    const int f = q | ((g & 1) << 2);
#pragma unroll
    for (int u = 0; u < 4; ++u) drd[u] = lbase + H * G2_STAGE + 8192 + (8 * g + q) * 256 + (((4 * wn2 + u) ^ f) * 32) + 8 * p;
  }
  f32x4 acc[4][4];
#pragma unroll
  for (int t = 0; t < 4; ++t)
#pragma unroll
    for (int u = 0; u < 4; ++u) acc[t][u] = (f32x4){0.f, 0.f, 0.f, 0.f};
  const unsigned hra = lbase + H * G2_STAGE + hrd;
  const unsigned hra2 = lbase + H * G2_STAGE + wm2 * 4096 + lane * 8;
  WAIT_V(8);
  asm volatile("" ::"v"(pf3));
  BAR();
  if (wid == 0 && lane < 16) __hip_atomic_store(fin, 0ull, __ATOMIC_RELAXED, __HIP_MEMORY_SCOPE_AGENT);
  STAMP_NOW();
  bf16x4 dla[4], dha[4], dlb[4], dhb[4];
  bf16x8 ha[4], hb2[4];
  {
#pragma unroll
    for (int u = 0; u < 4; ++u) { dla[u] = tr_read<0>(drd[u]); dha[u] = tr_read<1024>(drd[u]); }
    ha[0] = ld128<0>(hra); ha[1] = ld128<1024>(hra); ha[2] = ld128<2048>(hra); ha[3] = ld128<3072>(hra);
  }
  int rs = 1, ws = 0;
  auto step2 = [&](int i, auto IL_, auto VM_, auto RD_, bf16x8(&hc)[4], bf16x8(&hn)[4], bf16x4(&dl)[4], bf16x4(&dh)[4], bf16x4(&dln)[4],
                  bf16x4(&dhn)[4]) __attribute__((always_inline)) {
    constexpr int IL = decltype(IL_)::value, VM = decltype(VM_)::value, RD = decltype(RD_)::value;
    const unsigned so = rs * (2 * G2_STAGE);
    const int S = 2 * (i + 5) + H;
    const int t0 = wd_t0(S), t1 = t0 + wd_dt(S);
    lds_u8* l = lds + ws * (2 * G2_STAGE) + H * G2_STAGE;
    constexpr int LL = IL;
    bf16x8 df;
    WAIT_L(0);
    if (RD) {
      WAIT_V(VM);
      BAR();
    }
    SCHED();
    df = cat8(dl[0], dh[0]);
    MF(acc[0][0], df, hc[0]);
    if (RD) dln[0] = tr_read<0>(drd[0] + so);
    SCHED();
    MF(acc[1][0], df, hc[1]);
    if (RD) dhn[0] = tr_read<1024>(drd[0] + so);
    SCHED();
    MF(acc[2][0], df, hc[2]);
    if (RD == 1) hn[0] = ld128<0>(hra + so);
    if (RD == 2) hn[0] = ld2x64<0>(hra2 + so);
    SCHED();
    MF(acc[3][0], df, hc[3]);
    if (RD == 1) hn[1] = ld128<1024>(hra + so);
    if (RD == 2) hn[1] = ld2x64<2>(hra2 + so);
    SCHED();
    df = cat8(dl[1], dh[1]);
    MF(acc[0][1], df, hc[0]);
    if (RD) dln[1] = tr_read<0>(drd[1] + so);
    SCHED();
    MF(acc[1][1], df, hc[1]);
    if (RD) dhn[1] = tr_read<1024>(drd[1] + so);
    SCHED();
    MF(acc[2][1], df, hc[2]);
    if (RD == 1) hn[2] = ld128<2048>(hra + so);
    if (RD == 2) hn[2] = ld2x64<4>(hra2 + so);
    SCHED();
    MF(acc[3][1], df, hc[3]);
    if (RD == 1) hn[3] = ld128<3072>(hra + so);
    if (RD == 2) hn[3] = ld2x64<6>(hra2 + so);
    SCHED();
    df = cat8(dl[2], dh[2]);
    MF(acc[0][2], df, hc[0]);
    if (RD) dln[2] = tr_read<0>(drd[2] + so);
    SCHED();
    MF(acc[1][2], df, hc[1]);
    if (RD) dhn[2] = tr_read<1024>(drd[2] + so);
    SCHED();
    MF(acc[2][2], df, hc[2]);
    if (RD) dln[3] = tr_read<0>(drd[3] + so);
    SCHED();
    MF(acc[3][2], df, hc[3]);
    if (RD) dhn[3] = tr_read<1024>(drd[3] + so);
    SCHED();
    df = cat8(dl[3], dh[3]);
    MF(acc[0][3], df, hc[0]);
    if (LL) glds16c(hb + (size_t)S * (128 * 1024) + voffH, l + r * 2048);
    SCHED();
    MF(acc[1][3], df, hc[1]);
    if (LL) glds16c(hb + (size_t)S * (128 * 1024) + 1024 + voffH, l + r * 2048 + 1024);
    SCHED();
    MF(acc[2][3], df, hc[2]);
    if (LL) glds16(db + (size_t)t0 * (16 * D_MODEL * 2) + voffD, l + 8192 + r * 2048);
    SCHED();
    MF(acc[3][3], df, hc[3]);
    if (LL) glds16(db + (size_t)t1 * (16 * D_MODEL * 2) + voffD, l + 8192 + r * 2048 + 1024);
    SCHED();
    rs = (rs == 4) ? 0 : rs + 1;
    ws = (ws == 4) ? 0 : ws + 1;
  };
#define G2_STEP_A(i, a, b, c) step2(i, IC<a>{}, IC<b>{}, IC<c>{}, ha, hb2, dla, dha, dlb, dhb)
#define G2_STEP_B(i, a, b, c) step2(i, IC<a>{}, IC<b>{}, IC<c>{}, hb2, ha, dlb, dhb, dla, dha)
  unsigned long long pv = 0;
  auto poll_issue = [&](gu64* f) __attribute__((always_inline)) {
    asm volatile("global_load_dwordx2 %0, %1, off sc1" : "+v"(pv) : "v"(f) : "memory");
  };
  auto poll_check = [&](gu64* f) __attribute__((always_inline)) {
    asm volatile("" : "+v"(pv));
    if (!__all(pv == MAGIC)) wait_flags(f);
    if (lane < 16) __hip_atomic_store(f, 0ull, __ATOMIC_RELAXED, __HIP_MEMORY_SCOPE_AGENT);
    __builtin_amdgcn_s_waitcnt(0xC07F);
  };
  G2_STEP_A(0, 1, 6, 1);
  __builtin_amdgcn_raw_buffer_store_b64(hs[0], rs_h, hso, 0, 16);
  __builtin_amdgcn_raw_buffer_store_b64(hs[1], rs_h, hso + 1024, 0, 16);
  G2_STEP_B(1, 1, 10, 1);
  __builtin_amdgcn_raw_buffer_store_b64(hs[2], rs_h, hso + 2048, 0, 16);
  __builtin_amdgcn_raw_buffer_store_b64(hs[3], rs_h, hso + 3072, 0, 16);
  G2_STEP_A(2, 1, 14, 1);
  __builtin_amdgcn_raw_buffer_store_b64(hmx[0], rs_h, hm, 0, 16);
  __builtin_amdgcn_raw_buffer_store_b64(hmx[1], rs_h, hm + 2048, 0, 16);
  STAMP_NOW();
  G2_STEP_B(3, 1, 18, 1);
  G2_STEP_A(4, 1, 18, 1);
  G2_STEP_B(5, 1, 16, 1);
  G2_STEP_A(6, 1, 14, 1);
  G2_STEP_B(7, 1, 12, 1);
  if (wid == 0 && lane < 16) __hip_atomic_store(fout + 16, MAGIC, __ATOMIC_RELAXED, __HIP_MEMORY_SCOPE_AGENT);
  G2_STEP_A(8, 1, 12, 1);
  G2_STEP_B(9, 1, 12, 1);
  if (wid == 0) poll_issue(fin + 16);
  G2_STEP_A(10, 1, 12, 1);
  STAMP_NOW();
  if (wid == 0) {
    WAIT_V(4);
    poll_check(fin + 16);
  }
  asm volatile("" ::: "memory");
  STAMP_NOW();
  G2_STEP_B(11, 1, 12, 1);
  G2_STEP_A(12, 1, 12, 1);
  G2_STEP_B(13, 1, 12, 1);
  G2_STEP_A(14, 1, 12, 1);
  G2_STEP_B(15, 1, 12, 2);
  for (int i = 16; i < 22; i += 2) {
    G2_STEP_A(i, 1, 12, 2);
    G2_STEP_B(i + 1, 1, 12, 2);
  }
  G2_STEP_A(22, 1, 12, 2);
  G2_STEP_B(23, 0, 12, 2);
  G2_STEP_A(24, 0, 8, 2);
  G2_STEP_B(25, 0, 4, 2);
  G2_STEP_A(26, 0, 0, 2);
  G2_STEP_B(27, 0, 0, 0);
  BAR();
  STAMP_NOW();

  f32x4 k0[4], k1[4];
  {
    lds_u8* dst = lds + ((1 - H) * 4 + r) * 8192 + lane * 16;
    if (H) {
#pragma unroll
      for (int u = 0; u < 4; ++u) {
        *(LAS f32x4*)(dst + u * 1024) = acc[0][u];
        *(LAS f32x4*)(dst + (4 + u) * 1024) = acc[1][u];
        k0[u] = acc[2][u]; k1[u] = acc[3][u];
      }
    } else {
#pragma unroll
      for (int u = 0; u < 4; ++u) {
        *(LAS f32x4*)(dst + u * 1024) = acc[2][u];
        *(LAS f32x4*)(dst + (4 + u) * 1024) = acc[3][u];
        k0[u] = acc[0][u]; k1[u] = acc[1][u];
      }
    }
  }
  __syncthreads();
  f32x4 keep[4], send[4];
  {
    lds_u8* src = lds + wid * 8192 + lane * 16;
#pragma unroll
    for (int u = 0; u < 4; ++u) {
      k0[u] += *(LAS f32x4*)(src + u * 1024);
      k1[u] += *(LAS f32x4*)(src + (4 + u) * 1024);
    }
    if (kh) {
#pragma unroll
      for (int u = 0; u < 4; ++u) { keep[u] = k1[u]; send[u] = k0[u]; }
    } else {
#pragma unroll
      for (int u = 0; u < 4; ++u) { keep[u] = k0[u]; send[u] = k1[u]; }
    }
  }
  STAMP_NOW();
  {
    const int pb = bid ^ 2;
    auto rs_x = __builtin_amdgcn_make_buffer_rsrc(hbuf + WS_XCH, 0, 256 * 32768, 0x00020000);
    const unsigned so = (unsigned)(pb * 32768 + wid * 4096 + lane * 16);
#pragma unroll
    for (int u = 0; u < 4; ++u) __builtin_amdgcn_raw_buffer_store_b128(__builtin_bit_cast(u32x4, send[u]), rs_x, so + u * 1024, 0, 16);
    WAIT_V(0);
    BAR();
    STAMP_NOW();
    if (wid == 0) {
      gu64* fl_out = (gu64*)(hbuf + WS_FLAG + (size_t)pb * 512 + 384);
      gu64* fl_in = (gu64*)(hbuf + WS_FLAG + (size_t)bid * 512 + 384);
      if (lane == 0) __hip_atomic_store(fl_out, MAGIC, __ATOMIC_RELAXED, __HIP_MEMORY_SCOPE_AGENT);
      const unsigned long long t0 = __builtin_amdgcn_s_memrealtime();
      for (;;) {
        const bool ok = __hip_atomic_load(fl_in, __ATOMIC_RELAXED, __HIP_MEMORY_SCOPE_AGENT) == MAGIC;
        if (__all(ok)) break;
        if (__builtin_amdgcn_s_memrealtime() - t0 > 5000000ull) break;
        __builtin_amdgcn_s_sleep(1);
      }
      if (lane == 0) __hip_atomic_store(fl_in, 0ull, __ATOMIC_RELAXED, __HIP_MEMORY_SCOPE_AGENT);
    }
    asm volatile("" ::: "memory");
    BAR();
    asm volatile("" ::: "memory");
    const unsigned si = (unsigned)(bid * 32768 + wid * 4096 + lane * 16);
#pragma unroll
    for (int u = 0; u < 4; ++u) keep[u] += __builtin_bit_cast(f32x4, __builtin_amdgcn_raw_buffer_load_b128(rs_x, si + u * 1024, 0, 16));
  }
  unsigned char* op = out + ((size_t)(mt2 * 128 + wm2 * 64 + (2 * H + kh) * 16 + tok) * D_MODEL + nt2 * 128 + wn2 * 64) * 2;
#pragma unroll
  for (int u = 0; u < 4; u += 2) {
    unsigned a0 = cvt_pk_bf16(keep[u][0], keep[u][1]), a1 = cvt_pk_bf16(keep[u][2], keep[u][3]);
    unsigned b0 = cvt_pk_bf16(keep[u + 1][0], keep[u + 1][1]), b1 = cvt_pk_bf16(keep[u + 1][2], keep[u + 1][3]);
    auto s0 = __builtin_amdgcn_permlane16_swap(a0, b0, false, false);
    auto s1 = __builtin_amdgcn_permlane16_swap(a1, b1, false, false);
    u32x4 v = {s0[0], s1[0], s0[1], s1[1]};
    const int col = (g & 1) ? (16 * (u + 1) + 4 * (g - 1)) : (16 * u + 4 * g);
    *(u32x4*)(op + col * 2) = v;
  }
  STAMP_NOW();
  STAMP_NOW();
  STAMP_OUT(hbuf + (64u << 20));
}

extern "C" void kernel_launch(void* const* d_in, const int* in_sizes, int n_in, void* d_out, int out_size, void* d_ws,
                              size_t ws_size, hipStream_t stream) {
  const unsigned char* x = (const unsigned char*)d_in[0];
  const unsigned char* wg = (const unsigned char*)d_in[1];
  const unsigned char* wu = (const unsigned char*)d_in[2];
  const unsigned char* wd = (const unsigned char*)d_in[3];
  unsigned char* hbuf = (unsigned char*)d_ws;
  (void)hipFuncSetAttribute((const void*)k_fused, hipFuncAttributeMaxDynamicSharedMemorySize, G1_LDS);
  k_fused<<<256, 512, G1_LDS, stream>>>(x, wg, wu, hbuf, wd, (unsigned char*)d_out);
}
```

```cpp
#include <hip/hip_runtime.h>
#include <hip/hip_bf16.h>
#include <stdint.h>

typedef short bf16x8 __attribute__((ext_vector_type(8)));
typedef short bf16x4 __attribute__((ext_vector_type(4)));
typedef float f32x4 __attribute__((ext_vector_type(4)));
typedef float f32x2 __attribute__((ext_vector_type(2)));
typedef unsigned u32x2 __attribute__((ext_vector_type(2)));
typedef unsigned u32x4 __attribute__((ext_vector_type(4)));
#define LAS __attribute__((address_space(3)))
typedef LAS unsigned char lds_u8;

constexpr int M_TOK = 2048, D_MODEL = 1024, F_FF = 3584;
constexpr size_t WS_XCH = 16u << 20;
constexpr size_t WS_FLAG = 32u << 20;

#define WAIT_V(n) asm volatile("s_waitcnt vmcnt(%0)" ::"n"(n) : "memory")
#define WAIT_L0() asm volatile("s_waitcnt lgkmcnt(0)" ::: "memory")
#define BAR() __builtin_amdgcn_s_barrier()
#define SCHED() __builtin_amdgcn_sched_barrier(0)
#define FINL __device__ __forceinline__

template <int N> struct IC { static constexpr int value = N; };

FINL void glds16c(const void* g, lds_u8* l) {
  __builtin_amdgcn_global_load_lds((const unsigned*)g, (LAS unsigned*)l, 16, 0, 16);
}
FINL void glds16(const void* g, lds_u8* l) {
  __builtin_amdgcn_global_load_lds((const unsigned*)g, (LAS unsigned*)l, 16, 0, 0);
}
template <int OFF> FINL bf16x4 tr_read(unsigned addr) {
  bf16x4 r;
  asm volatile("ds_read_b64_tr_b16 %0, %1 offset:%2" : "=v"(r) : "v"(addr), "n"(OFF) : "memory");
  return r;
}
template <int OFF> FINL bf16x8 ld128(unsigned addr) {
  bf16x8 r;
  asm volatile("ds_read_b128 %0, %1 offset:%2" : "=v"(r) : "v"(addr), "n"(OFF) : "memory");
  return r;
}
template <int O0> FINL bf16x8 ld2x64(unsigned addr) {
  bf16x8 r;
  asm volatile("ds_read2st64_b64 %0, %1 offset0:%2 offset1:%3" : "=v"(r) : "v"(addr), "n"(O0), "n"(O0 + 1) : "memory");
  return r;
}
#define WAIT_L(n) asm volatile("s_waitcnt lgkmcnt(%0)" ::"n"(n) : "memory")
FINL bf16x8 cat8(bf16x4 a, bf16x4 b) { return __builtin_shufflevector(a, b, 0, 1, 2, 3, 4, 5, 6, 7); }
FINL unsigned cvt_pk_bf16(float lo, float hi) {
  unsigned r;
  asm("v_cvt_pk_bf16_f32 %0, %1, %2" : "=v"(r) : "v"(lo), "v"(hi));
  return r;
}
FINL f32x2 geglu2(f32x2 g, f32x2 u) {
  const float c0 = -2.0f * 0.7978845608028654f * 1.4426950408889634f;
  const float c1 = c0 * 0.044715f;
  f32x2 t = g * g;
  f32x2 p = t * c1 + c0;
  f32x2 a = g * p;
  f32x2 e;
  e.x = __builtin_amdgcn_exp2f(a.x);
  e.y = __builtin_amdgcn_exp2f(a.y);
  f32x2 d = e + 1.0f;
  f32x2 r;
  r.x = __builtin_amdgcn_rcpf(d.x);
  r.y = __builtin_amdgcn_rcpf(d.y);
  return (g * r) * u;
}
FINL u32x2 geglu4(f32x4 g, f32x4 u) {
  f32x2 a = geglu2((f32x2){g[0], g[1]}, (f32x2){u[0], u[1]});
  f32x2 b = geglu2((f32x2){g[2], g[3]}, (f32x2){u[2], u[3]});
  u32x2 o;
  o.x = cvt_pk_bf16(a.x, a.y);
  o.y = cvt_pk_bf16(b.x, b.y);
  return o;
}

constexpr int G1_WGRP = 2048;
constexpr int G1_WSLOT = 8 * G1_WGRP;
constexpr int G1_NW = 4;
constexpr int G1_XSLOT = 32768;
constexpr int G1_NX = 3;
constexpr int G1_XOFF = G1_NW * G1_WSLOT;
constexpr int G1_LDS = G1_XOFF + G1_NX * G1_XSLOT;

constexpr int G2_STAGE = 16384;
constexpr unsigned long long MAGIC = 0x5E1F0A3C97B2D461ull;
typedef __attribute__((address_space(1))) unsigned long long gu64;

__global__ __launch_bounds__(512, 2) void k_fused(const unsigned char* __restrict__ x, const unsigned char* __restrict__ wg,
                                                  const unsigned char* __restrict__ wu, unsigned char* hbuf,
                                                  const unsigned char* __restrict__ wd, unsigned char* __restrict__ out) {
  extern __shared__ __attribute__((aligned(16))) unsigned char smem_raw[];
  lds_u8* lds = (lds_u8*)smem_raw;
  const int tid = threadIdx.x;
  const int wid = __builtin_amdgcn_readfirstlane(tid >> 6);
  const int lane = tid & 63;
  const int half = wid >> 2, wm = wid & 3, wn = wid >> 2;
  const int bid = blockIdx.x;
  const int xcd = bid & 7, idx = bid >> 3;
  const int mt = (xcd >> 2) * 4 + (idx & 3);
  const int nt = (xcd & 3) * 8 + (idx >> 2);
  const int g = lane >> 4, q = (lane >> 2) & 3, p = lane & 3, tok = lane & 15;

  int li = lane - ((wid >> 1) & 1) * 8;
  li = (li < 0 || li >= 56) ? 0 : li;
  const unsigned voffW = (unsigned)((4 * wid + li / 14) * (F_FF * 2) + (li % 14) * 16);
  unsigned voffX[2];
#pragma unroll
  for (int i = 0; i < 2; ++i)
    voffX[i] = (unsigned)((8 * (2 * wid + i) + (lane >> 3)) * (D_MODEL * 2) + (((lane & 7) ^ ((i << 2) | (lane >> 4))) * 16));
  const unsigned char* wgb = wg + (size_t)nt * 224;
  const unsigned char* wub = wu + (size_t)nt * 224;
  const unsigned char* xb = x + (size_t)mt * 256 * (D_MODEL * 2);
  const int wdst = wid * G1_WGRP;
  const int xdst = G1_XOFF + wid * 2048;

  const int rot2 = (xcd >> 2) * 8;
  auto stageW = [&](int j, int slot) __attribute__((always_inline)) {
    const size_t go = (size_t)((j + 2 * rot2) & 31) * (32 * F_FF * 2);
    glds16(wgb + go + voffW, lds + slot * G1_WSLOT + wdst);
    glds16(wub + go + voffW, lds + slot * G1_WSLOT + wdst + 1024);
  };
  auto stageX = [&](int t, int hb, int slot) __attribute__((always_inline)) {
    const size_t go = (size_t)hb * (128 * D_MODEL * 2) + (size_t)((t + rot2) & 15) * 128;
    glds16(xb + go + voffX[0], lds + xdst + slot * G1_XSLOT + hb * 16384);
    glds16(xb + go + voffX[1], lds + xdst + slot * G1_XSLOT + hb * 16384 + 1024);
  };

  const unsigned lbase = (unsigned)(size_t)lds;
  const unsigned wrd = lbase + 2 * g * G1_WGRP + (g & 1) * 128 + q * 224 + p * 8 + wn * 128;
  const unsigned wrdm = lbase + 2 * g * G1_WGRP + (g & 1) * 128 + q * 224 + 96 + wn * 16 + (p & 1) * 8 + (p >> 1) * 1024;
  int xrd[2];
#pragma unroll
  for (int s = 0; s < 2; ++s) xrd[s] = G1_XOFF + (wm * 64 + tok) * 128 + ((((s << 2) | g) ^ ((tok >> 1) & 7)) * 16);

  f32x4 accg[4][3], accu[4][3], accm[4];
#pragma unroll
  for (int t = 0; t < 4; ++t) {
#pragma unroll
    for (int u = 0; u < 3; ++u) { accg[t][u] = (f32x4){0.f, 0.f, 0.f, 0.f}; accu[t][u] = (f32x4){0.f, 0.f, 0.f, 0.f}; }
    accm[t] = (f32x4){0.f, 0.f, 0.f, 0.f};
  }

  const int H = wid >> 2, r = wid & 3;
  const int kh = (xcd >> 1) & 1;
  const int mt2 = (xcd >> 2) * 8 + (idx & 7);
  const int nt2 = (xcd & 1) * 4 + (idx >> 3);
  const unsigned char* hb = hbuf + ((size_t)kh * 56 * 128 + mt2 * 8 + 2 * r) * 1024;
  const unsigned char* db = wd + (size_t)kh * 56 * 32 * (D_MODEL * 2) + (size_t)nt2 * 256;
  stageX(0, 0, 0); stageX(0, 1, 0); stageW(0, 0);
  stageW(1, 1); stageX(1, 0, 1);
  stageW(2, 2); stageX(1, 1, 1);
  stageW(3, 3); stageX(2, 0, 2);
  WAIT_V(12);
  BAR();

  const unsigned xra[2] = {lbase + (unsigned)xrd[0], lbase + (unsigned)xrd[1]};
  bf16x4 wl[7], wh[7];
  bf16x8 xa[4], xb2[4];
  {
    wl[0] = tr_read<0>(wrd); wh[0] = tr_read<G1_WGRP>(wrd);
    wl[1] = tr_read<32>(wrd); wh[1] = tr_read<32 + G1_WGRP>(wrd);
    wl[2] = tr_read<64>(wrd); wh[2] = tr_read<64 + G1_WGRP>(wrd);
    wl[3] = tr_read<1024>(wrd); wh[3] = tr_read<1024 + G1_WGRP>(wrd);
    wl[4] = tr_read<1024 + 32>(wrd); wh[4] = tr_read<1024 + 32 + G1_WGRP>(wrd);
    wl[5] = tr_read<1024 + 64>(wrd); wh[5] = tr_read<1024 + 64 + G1_WGRP>(wrd);
    wl[6] = tr_read<0>(wrdm); wh[6] = tr_read<G1_WGRP>(wrdm);
    xa[0] = ld128<0>(xra[0]); xa[1] = ld128<2048>(xra[0]); xa[2] = ld128<4096>(xra[0]); xa[3] = ld128<6144>(xra[0]);
    WAIT_L(0);
  }
  int xs_rd = 0, xs_wr = 2;
  auto step = [&](int k, auto S_, auto IW_, auto IX_, auto VM_, auto RD_, bf16x8(&xc)[4], bf16x8(&xn)[4], auto IP_) __attribute__((always_inline)) {
    constexpr int S = decltype(S_)::value;
    constexpr int IP = decltype(IP_)::value;
    constexpr int IW = decltype(IW_)::value, IX = decltype(IX_)::value, VM = decltype(VM_)::value, RD = decltype(RD_)::value;
    if (S) xs_rd = (xs_rd == 2) ? 0 : xs_rd + 1;
    const unsigned wb = ((k + 1) & 3) * G1_WSLOT + wrd;
    const unsigned wbm = ((k + 1) & 3) * G1_WSLOT + wrdm;
    const unsigned xbp = xs_rd * G1_XSLOT + xra[S ^ 1];
#define MF(ACC, WF, XF) ACC = __builtin_amdgcn_mfma_f32_16x16x32_bf16(WF, XF, ACC, 0, 0, 0); SCHED();
    constexpr int LW = IW, LX = IX;
    const size_t goW = (size_t)((k + 4 + 2 * rot2) & 31) * (32 * F_FF * 2);
    const size_t goX = (size_t)(S ^ 1) * (128 * D_MODEL * 2) + (size_t)((((k + 1) >> 1) + 2 + rot2) & 15) * 128;
    lds_u8* ldW = lds + (k & 3) * G1_WSLOT + wdst;
    lds_u8* ldX = lds + xdst + xs_wr * G1_XSLOT + (S ^ 1) * 16384;
    bf16x8 wf;
    WAIT_L(10);
    SCHED();
    wf = cat8(wl[0], wh[0]);
    MF(accg[0][0], wf, xc[0]);
    MF(accg[1][0], wf, xc[1]);
    MF(accg[2][0], wf, xc[2]);
    MF(accg[3][0], wf, xc[3]);
    WAIT_L(0);
    if (RD) {
      if (VM >= 0) WAIT_V(VM);
      BAR();
    }
    SCHED();
    wf = cat8(wl[3], wh[3]);
    MF(accu[0][0], wf, xc[0]);
    if (RD) wl[0] = tr_read<0>(wb);
    SCHED();
    MF(accu[1][0], wf, xc[1]);
    if (RD) wh[0] = tr_read<0 + G1_WGRP>(wb);
    SCHED();
    MF(accu[2][0], wf, xc[2]);
    if (RD) xn[0] = ld128<0>(xbp);
    SCHED();
    MF(accu[3][0], wf, xc[3]);
    if (RD) xn[1] = ld128<2048>(xbp);
    SCHED();
    wf = cat8(wl[1], wh[1]);
    MF(accg[0][1], wf, xc[0]);
    if (RD) wl[3] = tr_read<1024>(wb);
    SCHED();
    MF(accg[1][1], wf, xc[1]);
    if (RD) wh[3] = tr_read<1024 + G1_WGRP>(wb);
    SCHED();
    MF(accg[2][1], wf, xc[2]);
    if (RD) xn[2] = ld128<4096>(xbp);
    SCHED();
    MF(accg[3][1], wf, xc[3]);
    if (RD) xn[3] = ld128<6144>(xbp);
    SCHED();
    wf = cat8(wl[4], wh[4]);
    MF(accu[0][1], wf, xc[0]);
    if (RD) wl[1] = tr_read<32>(wb);
    SCHED();
    MF(accu[1][1], wf, xc[1]);
    if (RD) wh[1] = tr_read<32 + G1_WGRP>(wb);
    SCHED();
    MF(accu[2][1], wf, xc[2]);
    if (LW) glds16(wgb + goW + voffW, ldW);
    SCHED();
    MF(accu[3][1], wf, xc[3]);
    wf = cat8(wl[2], wh[2]);
    MF(accg[0][2], wf, xc[0]);
    if (RD) wl[4] = tr_read<1024 + 32>(wb);
    SCHED();
    MF(accg[1][2], wf, xc[1]);
    if (RD) wh[4] = tr_read<1024 + 32 + G1_WGRP>(wb);
    SCHED();
    MF(accg[2][2], wf, xc[2]);
    if (LW) glds16(wub + goW + voffW, ldW + 1024);
    SCHED();
    MF(accg[3][2], wf, xc[3]);
    wf = cat8(wl[5], wh[5]);
    MF(accu[0][2], wf, xc[0]);
    if (RD) wl[2] = tr_read<64>(wb);
    SCHED();
    MF(accu[1][2], wf, xc[1]);
    if (RD) wh[2] = tr_read<64 + G1_WGRP>(wb);
    SCHED();
    MF(accu[2][2], wf, xc[2]);
    if (LX) glds16(xb + goX + voffX[0], ldX);
    SCHED();
    MF(accu[3][2], wf, xc[3]);
    wf = cat8(wl[6], wh[6]);
    MF(accm[0], wf, xc[0]);
    if (RD) wl[5] = tr_read<1024 + 64>(wb);
    SCHED();
    MF(accm[1], wf, xc[1]);
    if (RD) wh[5] = tr_read<1024 + 64 + G1_WGRP>(wb);
    SCHED();
    MF(accm[2], wf, xc[2]);
    if (LX) glds16(xb + goX + voffX[1], ldX + 1024);
    SCHED();
    MF(accm[3], wf, xc[3]);
    if (RD) wl[6] = tr_read<0>(wbm);
    if (RD) wh[6] = tr_read<0 + G1_WGRP>(wbm);
    SCHED();
    if (!S) xs_wr = (xs_wr == 2) ? 0 : xs_wr + 1;
  };
  for (int k = 0; k < 26; k += 2) {
    step(k, IC<0>{}, IC<1>{}, IC<1>{}, IC<8>{}, IC<1>{}, xa, xb2, IC<0>{});
    step(k + 1, IC<1>{}, IC<1>{}, IC<1>{}, IC<8>{}, IC<1>{}, xb2, xa, IC<0>{});
  }
  step(26, IC<0>{}, IC<1>{}, IC<1>{}, IC<8>{}, IC<1>{}, xa, xb2, IC<0>{});
  step(27, IC<1>{}, IC<1>{}, IC<0>{}, IC<8>{}, IC<1>{}, xb2, xa, IC<0>{});
  {
    bf16x8 wf;
    WAIT_L(0);
    WAIT_V(6);
    BAR();
    SCHED();
    wf = cat8(wl[0], wh[0]);
    MF(accg[0][0], wf, xa[0]);
    xb2[0] = ld128<0>((xra[1] + 2 * G1_XSLOT));
    SCHED();
    MF(accg[1][0], wf, xa[1]);
    xb2[1] = ld128<2048>((xra[1] + 2 * G1_XSLOT));
    SCHED();
    MF(accg[2][0], wf, xa[2]);
    xb2[2] = ld128<4096>((xra[1] + 2 * G1_XSLOT));
    SCHED();
    MF(accg[3][0], wf, xa[3]);
    xb2[3] = ld128<6144>((xra[1] + 2 * G1_XSLOT));
    SCHED();
    wf = cat8(wl[3], wh[3]);
    MF(accu[0][0], wf, xa[0]);
    wl[0] = tr_read<0>((wrd + 1 * G1_WSLOT));
    SCHED();
    MF(accu[1][0], wf, xa[1]);
    wh[0] = tr_read<0 + G1_WGRP>((wrd + 1 * G1_WSLOT));
    SCHED();
    MF(accu[2][0], wf, xa[2]);
    MF(accu[3][0], wf, xa[3]);
    wf = cat8(wl[1], wh[1]);
    MF(accg[0][1], wf, xa[0]);
    wl[3] = tr_read<1024>((wrd + 1 * G1_WSLOT));
    SCHED();
    MF(accg[1][1], wf, xa[1]);
    wh[3] = tr_read<1024 + G1_WGRP>((wrd + 1 * G1_WSLOT));
    SCHED();
    MF(accg[2][1], wf, xa[2]);
    MF(accg[3][1], wf, xa[3]);
    wf = cat8(wl[4], wh[4]);
    MF(accu[0][1], wf, xa[0]);
    wl[1] = tr_read<32>((wrd + 1 * G1_WSLOT));
    SCHED();
    MF(accu[1][1], wf, xa[1]);
    wh[1] = tr_read<32 + G1_WGRP>((wrd + 1 * G1_WSLOT));
    SCHED();
    MF(accu[2][1], wf, xa[2]);
    MF(accu[3][1], wf, xa[3]);
    wl[4] = tr_read<1024 + 32>((wrd + 1 * G1_WSLOT));
    wh[4] = tr_read<1024 + 32 + G1_WGRP>((wrd + 1 * G1_WSLOT));
    SCHED();
  }
  {
    bf16x8 wf;
    WAIT_L(0);
    WAIT_V(2);
    BAR();
    SCHED();
    wf = cat8(wl[0], wh[0]);
    MF(accg[0][0], wf, xb2[0]);
    xa[0] = ld128<0>((xra[0] + 0 * G1_XSLOT));
    SCHED();
    MF(accg[1][0], wf, xb2[1]);
    xa[1] = ld128<2048>((xra[0] + 0 * G1_XSLOT));
    SCHED();
    MF(accg[2][0], wf, xb2[2]);
    xa[2] = ld128<4096>((xra[0] + 0 * G1_XSLOT));
    SCHED();
    MF(accg[3][0], wf, xb2[3]);
    xa[3] = ld128<6144>((xra[0] + 0 * G1_XSLOT));
    SCHED();
    wf = cat8(wl[3], wh[3]);
    MF(accu[0][0], wf, xb2[0]);
    wl[0] = tr_read<0>((wrd + 2 * G1_WSLOT));
    SCHED();
    MF(accu[1][0], wf, xb2[1]);
    wh[0] = tr_read<0 + G1_WGRP>((wrd + 2 * G1_WSLOT));
    SCHED();
    MF(accu[2][0], wf, xb2[2]);
    MF(accu[3][0], wf, xb2[3]);
    wf = cat8(wl[1], wh[1]);
    MF(accg[0][1], wf, xb2[0]);
    wl[3] = tr_read<1024>((wrd + 2 * G1_WSLOT));
    SCHED();
    MF(accg[1][1], wf, xb2[1]);
    wh[3] = tr_read<1024 + G1_WGRP>((wrd + 2 * G1_WSLOT));
    SCHED();
    MF(accg[2][1], wf, xb2[2]);
    MF(accg[3][1], wf, xb2[3]);
    wf = cat8(wl[4], wh[4]);
    MF(accu[0][1], wf, xb2[0]);
    wl[1] = tr_read<32>((wrd + 2 * G1_WSLOT));
    SCHED();
    MF(accu[1][1], wf, xb2[1]);
    wh[1] = tr_read<32 + G1_WGRP>((wrd + 2 * G1_WSLOT));
    SCHED();
    MF(accu[2][1], wf, xb2[2]);
    MF(accu[3][1], wf, xb2[3]);
    wl[4] = tr_read<1024 + 32>((wrd + 2 * G1_WSLOT));
    wh[4] = tr_read<1024 + 32 + G1_WGRP>((wrd + 2 * G1_WSLOT));
    SCHED();
  }
  {
    bf16x8 wf;
    WAIT_L(0);
    WAIT_V(0);
    BAR();
    SCHED();
    wf = cat8(wl[0], wh[0]);
    MF(accg[0][0], wf, xa[0]);
    xb2[0] = ld128<0>((xra[1] + 0 * G1_XSLOT));
    SCHED();
    MF(accg[1][0], wf, xa[1]);
    xb2[1] = ld128<2048>((xra[1] + 0 * G1_XSLOT));
    SCHED();
    MF(accg[2][0], wf, xa[2]);
    xb2[2] = ld128<4096>((xra[1] + 0 * G1_XSLOT));
    SCHED();
    MF(accg[3][0], wf, xa[3]);
    xb2[3] = ld128<6144>((xra[1] + 0 * G1_XSLOT));
    SCHED();
    wf = cat8(wl[3], wh[3]);
    MF(accu[0][0], wf, xa[0]);
    wl[0] = tr_read<0>((wrd + 3 * G1_WSLOT));
    SCHED();
    MF(accu[1][0], wf, xa[1]);
    wh[0] = tr_read<0 + G1_WGRP>((wrd + 3 * G1_WSLOT));
    SCHED();
    MF(accu[2][0], wf, xa[2]);
    MF(accu[3][0], wf, xa[3]);
    wf = cat8(wl[1], wh[1]);
    MF(accg[0][1], wf, xa[0]);
    wl[3] = tr_read<1024>((wrd + 3 * G1_WSLOT));
    SCHED();
    MF(accg[1][1], wf, xa[1]);
    wh[3] = tr_read<1024 + G1_WGRP>((wrd + 3 * G1_WSLOT));
    SCHED();
    MF(accg[2][1], wf, xa[2]);
    MF(accg[3][1], wf, xa[3]);
    wf = cat8(wl[4], wh[4]);
    MF(accu[0][1], wf, xa[0]);
    wl[1] = tr_read<32>((wrd + 3 * G1_WSLOT));
    SCHED();
    MF(accu[1][1], wf, xa[1]);
    wh[1] = tr_read<32 + G1_WGRP>((wrd + 3 * G1_WSLOT));
    SCHED();
    MF(accu[2][1], wf, xa[2]);
    MF(accu[3][1], wf, xa[3]);
    wl[4] = tr_read<1024 + 32>((wrd + 3 * G1_WSLOT));
    wh[4] = tr_read<1024 + 32 + G1_WGRP>((wrd + 3 * G1_WSLOT));
    SCHED();
  }
  unsigned pf3;
  {
    const int e2 = (wid < 7 ? wid : 6) * 64 + lane;
    const unsigned char* pw = wd + (size_t)(kh * 1792 + idx * 56 + (e2 >> 3)) * 2048 + (xcd & 1) * 1024 + (e2 & 7) * 128;
    asm volatile("global_load_dword %0, %1, off" : "=v"(pf3) : "v"(pw) : "memory");
  }
  {
    bf16x8 wf;
    WAIT_L(0);
    SCHED();
    wf = cat8(wl[0], wh[0]);
    MF(accg[0][0], wf, xb2[0]);
    xa[0] = ld128<0>((xra[0] + 2 * G1_XSLOT));
    SCHED();
    MF(accg[1][0], wf, xb2[1]);
    xa[1] = ld128<2048>((xra[0] + 2 * G1_XSLOT));
    SCHED();
    MF(accg[2][0], wf, xb2[2]);
    xa[2] = ld128<4096>((xra[0] + 2 * G1_XSLOT));
    SCHED();
    MF(accg[3][0], wf, xb2[3]);
    xa[3] = ld128<6144>((xra[0] + 2 * G1_XSLOT));
    SCHED();
    wf = cat8(wl[3], wh[3]);
    MF(accu[0][0], wf, xb2[0]);
    MF(accu[1][0], wf, xb2[1]);
    MF(accu[2][0], wf, xb2[2]);
    MF(accu[3][0], wf, xb2[3]);
    wf = cat8(wl[1], wh[1]);
    MF(accg[0][1], wf, xb2[0]);
    MF(accg[1][1], wf, xb2[1]);
    MF(accg[2][1], wf, xb2[2]);
    MF(accg[3][1], wf, xb2[3]);
    wf = cat8(wl[4], wh[4]);
    MF(accu[0][1], wf, xb2[0]);
    MF(accu[1][1], wf, xb2[1]);
    MF(accu[2][1], wf, xb2[2]);
    MF(accu[3][1], wf, xb2[3]);
    SCHED();
  }
  auto rs_h = __builtin_amdgcn_make_buffer_rsrc(hbuf, 0, 112 * 128 * 1024, 0x00020000);
  const int T0 = mt * 16 + wm * 4;
  const int sbase = (nt >> 4) * 56, jj = nt & 15;
  {
    const unsigned ho = (unsigned)(((sbase + 2 * jj + wn) * 128 + T0) * 1024 + lane * 16);
#pragma unroll
    for (int t = 0; t < 4; ++t) {
      const u32x2 a = geglu4(accg[t][0], accu[t][0]), b = geglu4(accg[t][1], accu[t][1]);
      __builtin_amdgcn_raw_buffer_store_b128((u32x4){a.x, a.y, b.x, b.y}, rs_h, ho + t * 1024, 0, 16);
    }
  }
  SCHED();
  {
    bf16x8 wf;
    WAIT_L(0);
    SCHED();
    wf = cat8(wl[2], wh[2]);
    MF(accg[0][2], wf, xa[0]);
    xb2[0] = ld128<0>((xra[1] + 2 * G1_XSLOT));
    SCHED();
    MF(accg[1][2], wf, xa[1]);
    xb2[1] = ld128<2048>((xra[1] + 2 * G1_XSLOT));
    SCHED();
    MF(accg[2][2], wf, xa[2]);
    xb2[2] = ld128<4096>((xra[1] + 2 * G1_XSLOT));
    SCHED();
    MF(accg[3][2], wf, xa[3]);
    xb2[3] = ld128<6144>((xra[1] + 2 * G1_XSLOT));
    SCHED();
    wf = cat8(wl[5], wh[5]);
    MF(accu[0][2], wf, xa[0]);
    wl[2] = tr_read<64>((wrd + 1 * G1_WSLOT));
    SCHED();
    MF(accu[1][2], wf, xa[1]);
    wh[2] = tr_read<64 + G1_WGRP>((wrd + 1 * G1_WSLOT));
    SCHED();
    MF(accu[2][2], wf, xa[2]);
    MF(accu[3][2], wf, xa[3]);
    wf = cat8(wl[6], wh[6]);
    MF(accm[0], wf, xa[0]);
    wl[5] = tr_read<1024 + 64>((wrd + 1 * G1_WSLOT));
    SCHED();
    MF(accm[1], wf, xa[1]);
    wh[5] = tr_read<1024 + 64 + G1_WGRP>((wrd + 1 * G1_WSLOT));
    SCHED();
    MF(accm[2], wf, xa[2]);
    MF(accm[3], wf, xa[3]);
    wl[6] = tr_read<0>((wrdm + 1 * G1_WSLOT));
    wh[6] = tr_read<0 + G1_WGRP>((wrdm + 1 * G1_WSLOT));
    SCHED();
  }
  {
    bf16x8 wf;
    WAIT_L(0);
    SCHED();
    wf = cat8(wl[2], wh[2]);
    MF(accg[0][2], wf, xb2[0]);
    xa[0] = ld128<0>((xra[0] + 0 * G1_XSLOT));
    SCHED();
    MF(accg[1][2], wf, xb2[1]);
    xa[1] = ld128<2048>((xra[0] + 0 * G1_XSLOT));
    SCHED();
    MF(accg[2][2], wf, xb2[2]);
    xa[2] = ld128<4096>((xra[0] + 0 * G1_XSLOT));
    SCHED();
    MF(accg[3][2], wf, xb2[3]);
    xa[3] = ld128<6144>((xra[0] + 0 * G1_XSLOT));
    SCHED();
    wf = cat8(wl[5], wh[5]);
    MF(accu[0][2], wf, xb2[0]);
    wl[2] = tr_read<64>((wrd + 2 * G1_WSLOT));
    SCHED();
    MF(accu[1][2], wf, xb2[1]);
    wh[2] = tr_read<64 + G1_WGRP>((wrd + 2 * G1_WSLOT));
    SCHED();
    MF(accu[2][2], wf, xb2[2]);
    MF(accu[3][2], wf, xb2[3]);
    wf = cat8(wl[6], wh[6]);
    MF(accm[0], wf, xb2[0]);
    wl[5] = tr_read<1024 + 64>((wrd + 2 * G1_WSLOT));
    SCHED();
    MF(accm[1], wf, xb2[1]);
    wh[5] = tr_read<1024 + 64 + G1_WGRP>((wrd + 2 * G1_WSLOT));
    SCHED();
    MF(accm[2], wf, xb2[2]);
    MF(accm[3], wf, xb2[3]);
    wl[6] = tr_read<0>((wrdm + 2 * G1_WSLOT));
    wh[6] = tr_read<0 + G1_WGRP>((wrdm + 2 * G1_WSLOT));
    SCHED();
  }
  {
    bf16x8 wf;
    WAIT_L(0);
    SCHED();
    wf = cat8(wl[2], wh[2]);
    MF(accg[0][2], wf, xa[0]);
    xb2[0] = ld128<0>((xra[1] + 0 * G1_XSLOT));
    SCHED();
    MF(accg[1][2], wf, xa[1]);
    xb2[1] = ld128<2048>((xra[1] + 0 * G1_XSLOT));
    SCHED();
    MF(accg[2][2], wf, xa[2]);
    xb2[2] = ld128<4096>((xra[1] + 0 * G1_XSLOT));
    SCHED();
    MF(accg[3][2], wf, xa[3]);
    xb2[3] = ld128<6144>((xra[1] + 0 * G1_XSLOT));
    SCHED();
    wf = cat8(wl[5], wh[5]);
    MF(accu[0][2], wf, xa[0]);
    wl[2] = tr_read<64>((wrd + 3 * G1_WSLOT));
    SCHED();
    MF(accu[1][2], wf, xa[1]);
    wh[2] = tr_read<64 + G1_WGRP>((wrd + 3 * G1_WSLOT));
    SCHED();
    MF(accu[2][2], wf, xa[2]);
    MF(accu[3][2], wf, xa[3]);
    wf = cat8(wl[6], wh[6]);
    MF(accm[0], wf, xa[0]);
    wl[5] = tr_read<1024 + 64>((wrd + 3 * G1_WSLOT));
    SCHED();
    MF(accm[1], wf, xa[1]);
    wh[5] = tr_read<1024 + 64 + G1_WGRP>((wrd + 3 * G1_WSLOT));
    SCHED();
    MF(accm[2], wf, xa[2]);
    MF(accm[3], wf, xa[3]);
    wl[6] = tr_read<0>((wrdm + 3 * G1_WSLOT));
    wh[6] = tr_read<0 + G1_WGRP>((wrdm + 3 * G1_WSLOT));
    SCHED();
  }
  {
    bf16x8 wf;
    WAIT_L(0);
    SCHED();
    wf = cat8(wl[2], wh[2]);
    MF(accg[0][2], wf, xb2[0]);
    MF(accg[1][2], wf, xb2[1]);
    MF(accg[2][2], wf, xb2[2]);
    MF(accg[3][2], wf, xb2[3]);
    wf = cat8(wl[5], wh[5]);
    MF(accu[0][2], wf, xb2[0]);
    MF(accu[1][2], wf, xb2[1]);
    MF(accu[2][2], wf, xb2[2]);
    MF(accu[3][2], wf, xb2[3]);
    wf = cat8(wl[6], wh[6]);
    MF(accm[0], wf, xb2[0]);
    MF(accm[1], wf, xb2[1]);
    MF(accm[2], wf, xb2[2]);
    MF(accm[3], wf, xb2[3]);
    SCHED();
  }
  WAIT_V(0);
  BAR();
  const unsigned voffH = (unsigned)(lane * 16);
  const unsigned voffD = (unsigned)((4 * r + g) * (D_MODEL * 2) + ((((tok >> 1) ^ (g | ((r & 1) << 2))) << 1) | (lane & 1)) * 16);
  gu64* fout;
  gu64* fin = (gu64*)(hbuf + WS_FLAG + (size_t)bid * 512 + (lane & 15) * 8);
  {
    const int cm = 2 * mt + (lane & 1), cn = (lane >> 1) & 7;
    const int cb = (((cm & 7) | ((cn & 3) << 3)) << 3) | ((cm >> 3) << 2) | ((nt >> 4) << 1) | (cn >> 2);
    fout = (gu64*)(hbuf + WS_FLAG + (size_t)cb * 512 + (nt & 15) * 8);
  }
  if (wid == 0 && lane < 16) __hip_atomic_store(fout, MAGIC, __ATOMIC_RELAXED, __HIP_MEMORY_SCOPE_AGENT);
  auto wd_t0 = [&](int sg) __attribute__((always_inline)) -> int {
    return sg < 32 ? 7 * (sg >> 1) + 4 * (sg & 1) : sg < 48 ? 7 * (sg - 32) + 2 : 14 * (sg - 48) + 3;
  };
  auto wd_dt = [&](int sg) __attribute__((always_inline)) -> int { return sg < 32 ? 1 : sg < 48 ? 4 : 7; };
#pragma unroll
  for (int i = 0; i < 5; ++i) {
    const int t0 = wd_t0(2 * i + H);
    lds_u8* l = lds + (2 * i + H) * G2_STAGE;
    glds16(db + (size_t)t0 * (16 * D_MODEL * 2) + voffD, l + 8192 + r * 2048);
    glds16(db + (size_t)(t0 + 1) * (16 * D_MODEL * 2) + voffD, l + 8192 + r * 2048 + 1024);
  }
  u32x2 hs[4], hmx[2];
#pragma unroll
  for (int t = 0; t < 4; ++t) hs[t] = geglu4(accg[t][2], accu[t][2]);
#pragma unroll
  for (int t = 0; t < 4; t += 2) {
    f32x4 gg, uu;
#pragma unroll
    for (int rr = 0; rr < 4; ++rr) {
      auto sw = __builtin_amdgcn_permlane32_swap(__float_as_uint(accm[t][rr]), __float_as_uint(accm[t + 1][rr]), false, false);
      gg[rr] = __uint_as_float(sw[0]);
      uu[rr] = __uint_as_float(sw[1]);
    }
    hmx[t >> 1] = geglu4(gg, uu);
  }
  asm volatile("" : "+v"(hs[0]), "+v"(hs[1]), "+v"(hs[2]), "+v"(hs[3]), "+v"(hmx[0]), "+v"(hmx[1]));
  auto wait_flags = [&](gu64* f) __attribute__((always_inline)) {
    const unsigned long long t0 = __builtin_amdgcn_s_memrealtime();
    for (;;) {
      const bool ok = __hip_atomic_load(f, __ATOMIC_RELAXED, __HIP_MEMORY_SCOPE_AGENT) == MAGIC;
      if (__all(ok)) break;
      if (__builtin_amdgcn_s_memrealtime() - t0 > 5000000ull) break;
      __builtin_amdgcn_s_sleep(1);
    }
  };
  if (wid == 0) {
    wait_flags(fin);
    __builtin_amdgcn_s_waitcnt(0xC07F);
  }
  asm volatile("" ::: "memory");
  BAR();
  asm volatile("" ::: "memory");

#pragma unroll
  for (int i = 0; i < 5; ++i) {
    const int S = 2 * i + H;
    lds_u8* l = lds + S * G2_STAGE;
    glds16c(hb + (size_t)S * (128 * 1024) + voffH, l + r * 2048);
    glds16c(hb + (size_t)S * (128 * 1024) + 1024 + voffH, l + r * 2048 + 1024);
  }
  const unsigned hso = (unsigned)(((sbase + 32 + jj) * 128 + T0) * 1024 + wn * 512 + lane * 8);
  const unsigned hm = (unsigned)(((sbase + 48 + (jj >> 1)) * 128 + T0 + (lane >> 5)) * 1024 + (jj & 1) * 512 + ((2 * wn + (g & 1)) * 16 + tok) * 8);
  const int wm2 = r & 1, wn2 = r >> 1;
  const int hrd = wm2 * 4096 + lane * 16;
  unsigned drd[4];
  {
    const int f = q | ((g & 1) << 2);
#pragma unroll
    for (int u = 0; u < 4; ++u) drd[u] = lbase + H * G2_STAGE + 8192 + (8 * g + q) * 256 + (((4 * wn2 + u) ^ f) * 32) + 8 * p;
  }
  f32x4 acc[4][4];
#pragma unroll
  for (int t = 0; t < 4; ++t)
#pragma unroll
    for (int u = 0; u < 4; ++u) acc[t][u] = (f32x4){0.f, 0.f, 0.f, 0.f};
  const unsigned hra = lbase + H * G2_STAGE + hrd;
  const unsigned hra2 = lbase + H * G2_STAGE + wm2 * 4096 + lane * 8;
  WAIT_V(8);
  asm volatile("" ::"v"(pf3));
  BAR();
  if (wid == 0 && lane < 16) __hip_atomic_store(fin, 0ull, __ATOMIC_RELAXED, __HIP_MEMORY_SCOPE_AGENT);
  bf16x4 dla[4], dha[4], dlb[4], dhb[4];
  bf16x8 ha[4], hb2[4];
  {
#pragma unroll
    for (int u = 0; u < 4; ++u) { dla[u] = tr_read<0>(drd[u]); dha[u] = tr_read<1024>(drd[u]); }
    ha[0] = ld128<0>(hra); ha[1] = ld128<1024>(hra); ha[2] = ld128<2048>(hra); ha[3] = ld128<3072>(hra);
  }
  int rs = 1, ws = 0;
  auto step2 = [&](int i, auto IL_, auto VM_, auto RD_, bf16x8(&hc)[4], bf16x8(&hn)[4], bf16x4(&dl)[4], bf16x4(&dh)[4], bf16x4(&dln)[4],
                  bf16x4(&dhn)[4]) __attribute__((always_inline)) {
    constexpr int IL = decltype(IL_)::value, VM = decltype(VM_)::value, RD = decltype(RD_)::value;
    const unsigned so = rs * (2 * G2_STAGE);
    const int S = 2 * (i + 5) + H;
    const int t0 = wd_t0(S), t1 = t0 + wd_dt(S);
    lds_u8* l = lds + ws * (2 * G2_STAGE) + H * G2_STAGE;
    constexpr int LL = IL;
    bf16x8 df;
    WAIT_L(0);
    if (RD) {
      WAIT_V(VM);
      BAR();
    }
    SCHED();
    df = cat8(dl[0], dh[0]);
    MF(acc[0][0], df, hc[0]);
    if (RD) dln[0] = tr_read<0>(drd[0] + so);
    SCHED();
    MF(acc[1][0], df, hc[1]);
    if (RD) dhn[0] = tr_read<1024>(drd[0] + so);
    SCHED();
    MF(acc[2][0], df, hc[2]);
    if (RD == 1) hn[0] = ld128<0>(hra + so);
    if (RD == 2) hn[0] = ld2x64<0>(hra2 + so);
    SCHED();
    MF(acc[3][0], df, hc[3]);
    if (RD == 1) hn[1] = ld128<1024>(hra + so);
    if (RD == 2) hn[1] = ld2x64<2>(hra2 + so);
    SCHED();
    df = cat8(dl[1], dh[1]);
    MF(acc[0][1], df, hc[0]);
    if (RD) dln[1] = tr_read<0>(drd[1] + so);
    SCHED();
    MF(acc[1][1], df, hc[1]);
    if (RD) dhn[1] = tr_read<1024>(drd[1] + so);
    SCHED();
    MF(acc[2][1], df, hc[2]);
    if (RD == 1) hn[2] = ld128<2048>(hra + so);
    if (RD == 2) hn[2] = ld2x64<4>(hra2 + so);
    SCHED();
    MF(acc[3][1], df, hc[3]);
    if (RD == 1) hn[3] = ld128<3072>(hra + so);
    if (RD == 2) hn[3] = ld2x64<6>(hra2 + so);
    SCHED();
    df = cat8(dl[2], dh[2]);
    MF(acc[0][2], df, hc[0]);
    if (RD) dln[2] = tr_read<0>(drd[2] + so);
    SCHED();
    MF(acc[1][2], df, hc[1]);
    if (RD) dhn[2] = tr_read<1024>(drd[2] + so);
    SCHED();
    MF(acc[2][2], df, hc[2]);
    if (RD) dln[3] = tr_read<0>(drd[3] + so);
    SCHED();
    MF(acc[3][2], df, hc[3]);
    if (RD) dhn[3] = tr_read<1024>(drd[3] + so);
    SCHED();
    df = cat8(dl[3], dh[3]);
    MF(acc[0][3], df, hc[0]);
    if (LL) glds16c(hb + (size_t)S * (128 * 1024) + voffH, l + r * 2048);
    SCHED();
    MF(acc[1][3], df, hc[1]);
    if (LL) glds16c(hb + (size_t)S * (128 * 1024) + 1024 + voffH, l + r * 2048 + 1024);
    SCHED();
    MF(acc[2][3], df, hc[2]);
    if (LL) glds16(db + (size_t)t0 * (16 * D_MODEL * 2) + voffD, l + 8192 + r * 2048);
    SCHED();
    MF(acc[3][3], df, hc[3]);
    if (LL) glds16(db + (size_t)t1 * (16 * D_MODEL * 2) + voffD, l + 8192 + r * 2048 + 1024);
    SCHED();
    rs = (rs == 4) ? 0 : rs + 1;
    ws = (ws == 4) ? 0 : ws + 1;
  };
#define G2_STEP_A(i, a, b, c) step2(i, IC<a>{}, IC<b>{}, IC<c>{}, ha, hb2, dla, dha, dlb, dhb)
#define G2_STEP_B(i, a, b, c) step2(i, IC<a>{}, IC<b>{}, IC<c>{}, hb2, ha, dlb, dhb, dla, dha)
  unsigned long long pv = 0;
  auto poll_issue = [&](gu64* f) __attribute__((always_inline)) {
    asm volatile("global_load_dwordx2 %0, %1, off sc1" : "+v"(pv) : "v"(f) : "memory");
  };
  auto poll_check = [&](gu64* f) __attribute__((always_inline)) {
    asm volatile("" : "+v"(pv));
    if (!__all(pv == MAGIC)) wait_flags(f);
    if (lane < 16) __hip_atomic_store(f, 0ull, __ATOMIC_RELAXED, __HIP_MEMORY_SCOPE_AGENT);
    __builtin_amdgcn_s_waitcnt(0xC07F);
  };
  G2_STEP_A(0, 1, 6, 1);
  __builtin_amdgcn_raw_buffer_store_b64(hs[0], rs_h, hso, 0, 16);
  __builtin_amdgcn_raw_buffer_store_b64(hs[1], rs_h, hso + 1024, 0, 16);
  G2_STEP_B(1, 1, 10, 1);
  __builtin_amdgcn_raw_buffer_store_b64(hs[2], rs_h, hso + 2048, 0, 16);
  __builtin_amdgcn_raw_buffer_store_b64(hs[3], rs_h, hso + 3072, 0, 16);
  G2_STEP_A(2, 1, 14, 1);
  __builtin_amdgcn_raw_buffer_store_b64(hmx[0], rs_h, hm, 0, 16);
  __builtin_amdgcn_raw_buffer_store_b64(hmx[1], rs_h, hm + 2048, 0, 16);
  G2_STEP_B(3, 1, 18, 1);
  G2_STEP_A(4, 1, 18, 1);
  G2_STEP_B(5, 1, 16, 1);
  G2_STEP_A(6, 1, 14, 1);
  G2_STEP_B(7, 1, 12, 1);
  if (wid == 0 && lane < 16) __hip_atomic_store(fout + 16, MAGIC, __ATOMIC_RELAXED, __HIP_MEMORY_SCOPE_AGENT);
  G2_STEP_A(8, 1, 12, 1);
  G2_STEP_B(9, 1, 12, 1);
  if (wid == 0) poll_issue(fin + 16);
  G2_STEP_A(10, 1, 12, 1);
  if (wid == 0) {
    WAIT_V(4);
    poll_check(fin + 16);
  }
  asm volatile("" ::: "memory");
  G2_STEP_B(11, 1, 12, 1);
  G2_STEP_A(12, 1, 12, 1);
  G2_STEP_B(13, 1, 12, 1);
  G2_STEP_A(14, 1, 12, 1);
  G2_STEP_B(15, 1, 12, 2);
  for (int i = 16; i < 22; i += 2) {
    G2_STEP_A(i, 1, 12, 2);
    G2_STEP_B(i + 1, 1, 12, 2);
  }
  G2_STEP_A(22, 1, 12, 2);
  G2_STEP_B(23, 0, 12, 2);
  G2_STEP_A(24, 0, 8, 2);
  G2_STEP_B(25, 0, 4, 2);
  G2_STEP_A(26, 0, 0, 2);
  G2_STEP_B(27, 0, 0, 0);
  BAR();

  f32x4 k0[4], k1[4];
  {
    lds_u8* dst = lds + ((1 - H) * 4 + r) * 8192 + lane * 16;
    if (H) {
#pragma unroll
      for (int u = 0; u < 4; ++u) {
        *(LAS f32x4*)(dst + u * 1024) = acc[0][u];
        *(LAS f32x4*)(dst + (4 + u) * 1024) = acc[1][u];
        k0[u] = acc[2][u]; k1[u] = acc[3][u];
      }
    } else {
#pragma unroll
      for (int u = 0; u < 4; ++u) {
        *(LAS f32x4*)(dst + u * 1024) = acc[2][u];
        *(LAS f32x4*)(dst + (4 + u) * 1024) = acc[3][u];
        k0[u] = acc[0][u]; k1[u] = acc[1][u];
      }
    }
  }
  __syncthreads();
  f32x4 keep[4], send[4];
  {
    lds_u8* src = lds + wid * 8192 + lane * 16;
#pragma unroll
    for (int u = 0; u < 4; ++u) {
      k0[u] += *(LAS f32x4*)(src + u * 1024);
      k1[u] += *(LAS f32x4*)(src + (4 + u) * 1024);
    }
    if (kh) {
#pragma unroll
      for (int u = 0; u < 4; ++u) { keep[u] = k1[u]; send[u] = k0[u]; }
    } else {
#pragma unroll
      for (int u = 0; u < 4; ++u) { keep[u] = k0[u]; send[u] = k1[u]; }
    }
  }
  {
    const int pb = bid ^ 2;
    auto rs_x = __builtin_amdgcn_make_buffer_rsrc(hbuf + WS_XCH, 0, 256 * 32768, 0x00020000);
    const unsigned so = (unsigned)(pb * 32768 + wid * 4096 + lane * 16);
#pragma unroll
    for (int u = 0; u < 4; ++u) __builtin_amdgcn_raw_buffer_store_b128(__builtin_bit_cast(u32x4, send[u]), rs_x, so + u * 1024, 0, 16);
    WAIT_V(0);
    BAR();
    if (wid == 0) {
      gu64* fl_out = (gu64*)(hbuf + WS_FLAG + (size_t)pb * 512 + 384);
      gu64* fl_in = (gu64*)(hbuf + WS_FLAG + (size_t)bid * 512 + 384);
      if (lane == 0) __hip_atomic_store(fl_out, MAGIC, __ATOMIC_RELAXED, __HIP_MEMORY_SCOPE_AGENT);
      const unsigned long long t0 = __builtin_amdgcn_s_memrealtime();
      for (;;) {
        const bool ok = __hip_atomic_load(fl_in, __ATOMIC_RELAXED, __HIP_MEMORY_SCOPE_AGENT) == MAGIC;
        if (__all(ok)) break;
        if (__builtin_amdgcn_s_memrealtime() - t0 > 5000000ull) break;
        __builtin_amdgcn_s_sleep(1);
      }
      if (lane == 0) __hip_atomic_store(fl_in, 0ull, __ATOMIC_RELAXED, __HIP_MEMORY_SCOPE_AGENT);
    }
    asm volatile("" ::: "memory");
    BAR();
    asm volatile("" ::: "memory");
    const unsigned si = (unsigned)(bid * 32768 + wid * 4096 + lane * 16);
#pragma unroll
    for (int u = 0; u < 4; ++u) keep[u] += __builtin_bit_cast(f32x4, __builtin_amdgcn_raw_buffer_load_b128(rs_x, si + u * 1024, 0, 16));
  }
  unsigned char* op = out + ((size_t)(mt2 * 128 + wm2 * 64 + (2 * H + kh) * 16 + tok) * D_MODEL + nt2 * 128 + wn2 * 64) * 2;
#pragma unroll
  for (int u = 0; u < 4; u += 2) {
    unsigned a0 = cvt_pk_bf16(keep[u][0], keep[u][1]), a1 = cvt_pk_bf16(keep[u][2], keep[u][3]);
    unsigned b0 = cvt_pk_bf16(keep[u + 1][0], keep[u + 1][1]), b1 = cvt_pk_bf16(keep[u + 1][2], keep[u + 1][3]);
    asm volatile("s_nop 1" : "+v"(a0), "+v"(a1), "+v"(b0), "+v"(b1));
    auto s0 = __builtin_amdgcn_permlane16_swap(a0, b0, false, false);
    auto s1 = __builtin_amdgcn_permlane16_swap(a1, b1, false, false);
    u32x4 v = {s0[0], s1[0], s0[1], s1[1]};
    const int col = (g & 1) ? (16 * (u + 1) + 4 * (g - 1)) : (16 * u + 4 * g);
    *(u32x4*)(op + col * 2) = v;
  }
}

extern "C" void kernel_launch(void* const* d_in, const int* in_sizes, int n_in, void* d_out, int out_size, void* d_ws,
                              size_t ws_size, hipStream_t stream) {
  const unsigned char* x = (const unsigned char*)d_in[0];
  const unsigned char* wg = (const unsigned char*)d_in[1];
  const unsigned char* wu = (const unsigned char*)d_in[2];
  const unsigned char* wd = (const unsigned char*)d_in[3];
  unsigned char* hbuf = (unsigned char*)d_ws;
  (void)hipFuncSetAttribute((const void*)k_fused, hipFuncAttributeMaxDynamicSharedMemorySize, G1_LDS);
  k_fused<<<256, 512, G1_LDS, stream>>>(x, wg, wu, hbuf, wd, (unsigned char*)d_out);
}
```
